```python
import torch

def _softmax(x, axis=-1):
    return torch.softmax(x, dim=axis)
def _gelu(x):
    return torch.nn.functional.gelu(x, approximate="tanh")
def _silu(x):
    return torch.nn.functional.silu(x)
def _sigmoid(x):
    return torch.sigmoid(x)
def _layernorm(x, axis=-1, eps=1e-5):
    m = x.mean(dim=axis, keepdim=True)
    v = x.var(dim=axis, keepdim=True, unbiased=False)
    return (x - m) / torch.sqrt(v + eps)
def _scatter_add(tgt, idx, src, axis=0):
    return tgt.clone().index_add_(axis, idx, src)
def _segment_sum(src, seg, n):
    out = torch.zeros((n,) + src.shape[1:], dtype=src.dtype)
    return out.index_add_(0, seg, src)
def _pad(x, pw):
    flat = tuple(v for pair in reversed(pw) for v in pair)
    return torch.nn.functional.pad(x, flat)

kernel_name = 'parallel_sum__sliding_block_cached__glu_gated_swiglu__decode'

def setup_inputs():

    _w = (lambda a: (lambda t: t.to(torch.bfloat16) if t.is_floating_point() else t)(torch.as_tensor(a)))
    def _A_setup():
        B, Sq, D = 4, 1, 640
        Hq, Hkv, Sk, Dh = 32, 32, 8192, 192
        assert Hq % Hkv == 0, (Hq, Hkv)
        return {
            "x": _w((lambda *s: torch.randn(*s, dtype=torch.bfloat16) * 0.05)(B, Sq, D)),
            "k_cache": _w((lambda *s: torch.randn(*s, dtype=torch.bfloat16) * 1.6)(B, Hkv, Sk, Dh)),
            "v_cache": _w((lambda *s: torch.randn(*s, dtype=torch.bfloat16) * 0.8)(B, Hkv, Sk, Dh)),
            "wq": _w((lambda *s: torch.randn(*s, dtype=torch.bfloat16) * 1.6)(D, Hq * Dh) * (D ** -0.5)),
            "wo": _w((lambda *s: torch.randn(*s, dtype=torch.bfloat16) * 0.8)(Hq * Dh, D) * ((Hq * Dh) ** -0.5)),
        }
    _A_in = _A_setup()

    def _B_setup():
        B, S, D, F = 4, 1, 640, 1536
        return {
            "x": _w((lambda *s: torch.randn(*s, dtype=torch.bfloat16) * 0.05)(B, S, D)),
            "w1": _w((lambda *s: torch.randn(*s, dtype=torch.bfloat16) * 0.4)(D, F) * (2.0 / D) ** 0.5),
            "w3": _w((lambda *s: torch.randn(*s, dtype=torch.bfloat16) * 0.4)(D, F) * (2.0 / D) ** 0.5),
            "w2": _w((lambda *s: torch.randn(*s, dtype=torch.bfloat16) * 0.4)(F, D) * (2.0 / F) ** 0.5),
        }
    _B_in = _B_setup()

    return {
        "A_x": _A_in["x"],
        "A_k_cache": _A_in["k_cache"],
        "A_v_cache": _A_in["v_cache"],
        "A_wq": _A_in["wq"],
        "A_wo": _A_in["wo"],
        "B_x": _B_in["x"],
        "B_w1": _B_in["w1"],
        "B_w3": _B_in["w3"],
        "B_w2": _B_in["w2"],
    }

def reference(A_x, A_k_cache, A_v_cache, A_wq, A_wo, B_x, B_w1, B_w3, B_w2):
    def _A_ref(x, k_cache, v_cache, wq, wo):
        B, Sq, D = x.shape
        Hq, Dh = 32, 192
        Hkv, Sk = k_cache.shape[1], k_cache.shape[2]
        W = min(4096, max(256, Sk // 4))
        rep = Hq // Hkv
        q = (lambda x, *d: x.permute(*d))((x @ wq).reshape(B, Sq, Hq, Dh), 0, 2, 1, 3)
        k = (lambda x, n, axis: torch.repeat_interleave(x, n, dim=axis))(k_cache[:, :, Sk - W:, :], rep, axis=1)
        v = (lambda x, n, axis: torch.repeat_interleave(x, n, dim=axis))(v_cache[:, :, Sk - W:, :], rep, axis=1)
        scores = torch.einsum("bhqd,bhkd->bhqk", q, k) * (Dh ** -0.5)
        p = _softmax(scores, axis=-1)
        o = torch.einsum("bhqk,bhkd->bhqd", p, v)
        return (lambda x, *d: x.permute(*d))(o, 0, 2, 1, 3).reshape(B, Sq, Hq * Dh) @ wo

    def _B_ref(x, w1, w3, w2):
        gate = torch.einsum("bsd,df->bsf", x, w1)
        up = torch.einsum("bsd,df->bsf", x, w3)
        h = _silu(gate) * up
        out = torch.einsum("bsf,fd->bsd", h, w2)
        return out

    x = A_x
    xn = _layernorm(x, axis=-1)
    return x + _A_ref(xn, A_k_cache, A_v_cache, A_wq, A_wo) + _B_ref(xn, B_w1, B_w3, B_w2)
```

```cpp
#include <hip/hip_runtime.h>
#include <hip/hip_bf16.h>
#include <cstdint>
#include <type_traits>

using bf16 = __hip_bfloat16;
using u32x2 = __attribute__((ext_vector_type(2))) unsigned;
using u32x4 = __attribute__((ext_vector_type(4))) unsigned;
using u32x16 = __attribute__((ext_vector_type(16))) unsigned;
using u32x8 = __attribute__((ext_vector_type(8))) unsigned;
using s16x4 = __attribute__((ext_vector_type(4))) short;
using f32x2 = __attribute__((ext_vector_type(2))) float;
using f32x4 = __attribute__((ext_vector_type(4))) float;
using bf16x2 = __attribute__((ext_vector_type(2))) __bf16;
using bf16x8 = __attribute__((ext_vector_type(8))) __bf16;
typedef unsigned long long u64;

constexpr int NB = 4, DM = 640, NH = 32, DH = 192, SK = 8192, WIN = 2048, FF = 1536;
constexpr float LOG2E = 1.4426950408889634f;
constexpr float SCALE = 0.07216878364870322f;
constexpr int WINL = 4;
constexpr int WINV = 12;
constexpr int TSW = 24;
constexpr int QBACK = 16;
constexpr int TAU = 83;
constexpr int TK = 7;
constexpr int TAU2 = 80;
constexpr int PR1 = 60;
constexpr int PR2 = 150;
constexpr int PR3 = 300;
constexpr int PRS = 3;
constexpr int CPE = 1;
constexpr int CPL = 1;
constexpr int CODEB = 29440;
constexpr int PT1 = 200;
constexpr int PT2 = 380;
constexpr int EPB = 1;
constexpr int SPAN = 80;
constexpr int WD2 = 12;
constexpr int WCH = 4;
constexpr int TOFS = 180;
constexpr int TDL1 = 8;
constexpr int TDL2 = 32;
constexpr int RING = 9;
constexpr int PSL = 6;
constexpr int HGT = 12;
constexpr int LATE = 100;
constexpr int WINB = 8;
constexpr int MLD = 330;
constexpr int OB = 70;
constexpr int OK8 = 7;
constexpr int PA = 235;
constexpr int EAG = 3;
constexpr int WD1 = 16;
constexpr int QTL = 6;
constexpr int QOF = 60;
constexpr int KORD = 0x3210;
constexpr int VORD = 0x3210;
constexpr int XD = 5;
constexpr int XV = 5;
constexpr int PTL = 4;
constexpr int POF = 15;
constexpr int PWN = 25;
constexpr int XM = 2;
constexpr int NCK = 4;
constexpr int CK0 = 7;
constexpr int CKP = 5;
constexpr int CKR = 3;
constexpr int CKE = 5;
constexpr int LTH = 10;
constexpr int KG = 64;
constexpr int DMX = 8;
constexpr int RSP = 6;
constexpr int CKS = 5;
constexpr int KI = 20;
constexpr int BPOS = 4;
constexpr int WTA = 0;
constexpr int SF1 = 1;
constexpr int SF2 = 0;
constexpr int WDF = 16;
constexpr int PRJ = 1;
constexpr int FL = 3;
constexpr int PRB = 1;
constexpr int PSL1 = 6;
constexpr int bat(int t) { return ((t < 16 ? KORD : VORD) >> (t & 12)) & 3; }
constexpr int kpos(int b) { return bat(0) == b ? 0 : bat(4) == b ? 4 : bat(8) == b ? 8 : 12; }
constexpr int V2 = bat(16) == 2 ? 16 : bat(20) == 2 ? 20 : bat(24) == 2 ? 24 : 28;
constexpr int cl4(int v) { return v < 0 ? 0 : v > 4 ? 4 : v; }
constexpr int sched(int t) { return (t <= TK ? TAU * t : TAU * TK + TAU2 * (t - TK)) + XD * cl4(t - kpos(2)) + XV * cl4(t - V2) - XM * (t > 12 ? (t > 24 ? 12 : t - 12) : 0) / 4; }
constexpr int vdone(int u) { return EAG ? (u == 25 ? 1 : u == 26 ? 3 : u == 27 ? 7 : u - 15 - EAG) : u - 24; }
constexpr int NBUF = RING;
constexpr int NT = 16;
constexpr int TSTR = 8 * 8 * 8 * DH * 2;
constexpr int BSTR = NH * SK * DH * 2;

constexpr int WS_EPOCH = 0;
constexpr int WS_Q = 256;
constexpr int WS_GU = WS_Q + 3 * NB * NH * DH * 4;
constexpr int WS_ML = WS_GU + 2 * NB * 2 * FF * 4;
constexpr int WS_OR = WS_ML + NH * 8 * 128;
constexpr int WS_BC = WS_OR + NH * 1024;
constexpr int WS_O = WS_BC + 8 * 4096;
constexpr int WS_Y = WS_O + NH * NB * 8 * DH * 2;
constexpr int WS_END = WS_Y + 5 * (NH + 8) * 128 * NB * 4;

__device__ __forceinline__ float dot2(unsigned k, unsigned q, float acc) {
  return __builtin_amdgcn_fdot2_f32_bf16(__builtin_bit_cast(bf16x2, k), __builtin_bit_cast(bf16x2, q), acc, false);
}
__device__ __forceinline__ float bflo(unsigned v) { return __builtin_bit_cast(float, v << 16); }
__device__ __forceinline__ float bfhi(unsigned v) { return __builtin_bit_cast(float, v & 0xffff0000u); }
template <int CTRL> __device__ __forceinline__ float dpp(float x) {
  return __builtin_bit_cast(float, __builtin_amdgcn_mov_dpp(__builtin_bit_cast(int, x), CTRL, 0xf, 0xf, true));
}
constexpr int XOR1 = 0xB1, XOR2 = 0x4E, XOR7 = 0x141, XOR8 = 0x128;
__device__ __forceinline__ float x16sum(float x) {
  auto s = __builtin_amdgcn_permlane16_swap(__float_as_uint(x), __float_as_uint(x), false, false);
  return __uint_as_float(s[0]) + __uint_as_float(s[1]);
}
__device__ __forceinline__ float x32sum(float x) {
  auto t = __builtin_amdgcn_permlane32_swap(__float_as_uint(x), __float_as_uint(x), false, false);
  return __uint_as_float(t[0]) + __uint_as_float(t[1]);
}
__device__ __forceinline__ float x16max(float x) {
  auto s = __builtin_amdgcn_permlane16_swap(__float_as_uint(x), __float_as_uint(x), false, false);
  return fmaxf(__uint_as_float(s[0]), __uint_as_float(s[1]));
}
__device__ __forceinline__ float x32max(float x) {
  auto t = __builtin_amdgcn_permlane32_swap(__float_as_uint(x), __float_as_uint(x), false, false);
  return fmaxf(__uint_as_float(t[0]), __uint_as_float(t[1]));
}
__device__ __forceinline__ float sum_kk(float x) { x += dpp<XOR8>(x); x = x16sum(x); return x32sum(x); }
__device__ __forceinline__ float wave_sum(float x) {
  x += dpp<XOR1>(x); x += dpp<XOR2>(x); x += dpp<XOR7>(x); return sum_kk(x);
}
__device__ __forceinline__ float tr32(float a, float b) {
  auto t = __builtin_amdgcn_permlane32_swap(__float_as_uint(a), __float_as_uint(b), false, false);
  return __uint_as_float(t[0]) + __uint_as_float(t[1]);
}
__device__ __forceinline__ float tr16(float a, float b) {
  auto t = __builtin_amdgcn_permlane16_swap(__float_as_uint(a), __float_as_uint(b), false, false);
  return __uint_as_float(t[0]) + __uint_as_float(t[1]);
}
__device__ __forceinline__ float tr8(float a, float b, bool hi) { return (hi ? b : a) + dpp<XOR8>(hi ? a : b); }
__device__ __forceinline__ unsigned cvtpk(float lo, float hi) {
  return __builtin_bit_cast(unsigned, __builtin_convertvector(f32x2{lo, hi}, bf16x2));
}
__device__ __forceinline__ float rbf(float v) { return bflo(cvtpk(v, 0.f)); }
__device__ __forceinline__ f32x4 mfma(u32x4 a, u32x4 b, f32x4 c) {
  return __builtin_amdgcn_mfma_f32_16x16x32_bf16(__builtin_bit_cast(bf16x8, a), __builtin_bit_cast(bf16x8, b), c, 0, 0, 0);
}
__device__ __forceinline__ u32x4 bfrag(const u32x4* r, int ec) {
  u32x4 f;
#pragma unroll
  for (int p = 0; p < 4; ++p) f[p] = __builtin_amdgcn_perm(r[2 * p + 1][ec >> 1], r[2 * p][ec >> 1], (ec & 1) ? 0x07060302u : 0x05040100u);
  return f;
}
typedef __amdgpu_buffer_rsrc_t rsrc_t;
__device__ __forceinline__ rsrc_t mkrs(const void* p, unsigned n) { return __builtin_amdgcn_make_buffer_rsrc(const_cast<void*>(p), (short)0, (int)n, 0x00020000); }
constexpr int AUX_NT = 2, AUX_SC1 = 16;
__device__ __forceinline__ u32x4 ldb(rsrc_t rs, int voff, int soff) { return __builtin_amdgcn_raw_buffer_load_b128(rs, voff, soff, 0); }
__device__ __forceinline__ u32x4 ldb_nt(rsrc_t rs, int voff, int soff) { return __builtin_amdgcn_raw_buffer_load_b128(rs, voff, soff, AUX_NT); }
__device__ __forceinline__ u32x4 ld_ag(rsrc_t rs, int voff) { return __builtin_amdgcn_raw_buffer_load_b128(rs, voff, 0, AUX_SC1); }
__device__ __forceinline__ void st_ag(rsrc_t rs, int voff, u32x4 v) { __builtin_amdgcn_raw_buffer_store_b128(v, rs, voff, 0, AUX_SC1); }
struct Tag { unsigned t0, t1; };
__device__ __forceinline__ unsigned r22(float v) { return (__float_as_uint(v) + 0x200u) >> 10; }
__device__ __forceinline__ float rnd22(float v) { return __uint_as_float(r22(v) << 10); }
__device__ __forceinline__ u32x4 g_pack(float a, float b, float c, float d, Tag t) {
  const unsigned b2 = r22(b), d2 = r22(d);
  return u32x4{r22(a) | (b2 << 22), (b2 >> 10) | t.t0, r22(c) | (d2 << 22), (d2 >> 10) | t.t1};
}
__device__ __forceinline__ bool g_ok(u32x4 g, Tag t) { return (((g[1] ^ t.t0) | (g[3] ^ t.t1)) >> 12) == 0u; }
__device__ __forceinline__ f32x4 g_val(u32x4 g) {
  return f32x4{__uint_as_float(g[0] << 10), __uint_as_float(__builtin_amdgcn_alignbit(g[1], g[0], 22) << 10),
               __uint_as_float(g[2] << 10), __uint_as_float(__builtin_amdgcn_alignbit(g[3], g[2], 22) << 10)};
}
__device__ __forceinline__ u32x2 h_pack(float a, float b, unsigned t) {
  const int e = max(__float_as_uint(a) & 0x7fffffffu, __float_as_uint(b) & 0x7fffffffu) >> 23;
  const unsigned ma = min((int)__builtin_rintf(__builtin_ldexpf(a, 143 - e)), 131071), mb = min((int)__builtin_rintf(__builtin_ldexpf(b, 143 - e)), 131071);
  return u32x2{(ma & 0x3ffffu) | (mb << 18), ((mb >> 14) & 15u) | (e << 4) | t};
}
__device__ __forceinline__ u32x4 p_pack(f32x4 v, Tag t) {
  const u32x2 lo = h_pack(v[0], v[1], t.t0), hi = h_pack(v[2], v[3], t.t1);
  return u32x4{lo[0], lo[1], hi[0], hi[1]};
}
__device__ __forceinline__ f32x4 p_val(u32x4 g) {
  f32x4 v;
#pragma unroll
  for (int i = 0; i < 4; i += 2) {
    const int e = (int)((g[i + 1] >> 4) & 255u) - 143;
    v[i] = __builtin_ldexpf((float)((int)(g[i] << 14) >> 14), e);
    v[i + 1] = __builtin_ldexpf((float)((int)(__builtin_amdgcn_alignbit(g[i + 1], g[i], 18) << 14) >> 14), e);
  }
  return v;
}
__device__ __forceinline__ unsigned s_exp(float a, float b) { return max(max(__float_as_uint(a) & 0x7f800000u, __float_as_uint(b) & 0x7f800000u), 8u << 23); }
__device__ __forceinline__ unsigned s_two(float a, float b, unsigned ex) {
  const float sc = __uint_as_float(((261u << 23) | 0x7f8000u) - ex);
  return (__float_as_uint(fmaf(a, sc, 12582912.f)) & 0x3ffu) | ((__float_as_uint(fmaf(b, sc, 12582912.f)) & 0x3ffu) << 10);
}
__device__ __forceinline__ void s_acc(float (&o)[8], u32x4 g, float sc, bool first) {
  sc *= __uint_as_float((((g[1] >> 8) & 15u) | ((g[3] >> 4) & 0xf0u)) << 23);
#pragma unroll
  for (int k = 0; k < 2; ++k)
#pragma unroll
    for (int i = 0; i < 4; ++i) {
      const float m = (float)(int)__builtin_amdgcn_sbfe(i < 3 ? g[2 * k] : __builtin_amdgcn_alignbit(g[2 * k + 1], g[2 * k], 30), i < 3 ? 10 * i : 0, 10);
      o[4 * k + i] = first ? m * sc : fmaf(m, sc, o[4 * k + i]);
    }
}
constexpr int SPIN_MAX = 1 << 18;
#define SB() __builtin_amdgcn_sched_barrier(0)
#define FENCE() asm volatile("" ::: "memory")
#define BAR() asm volatile("s_waitcnt lgkmcnt(0)\n\ts_barrier" ::: "memory")
#define VMW(n) asm volatile("s_waitcnt vmcnt(%0)" :: "n"(n) : "memory")
template <int SL, int SF> __device__ __forceinline__ void poll2(u32x4 (&r)[2], u32x4 (&p)[6], const int (&f)[2], rsrc_t rs, Tag tag) {
  unsigned t, cnt;
  asm volatile(
      ".if %[sf]\n\t"
      "buffer_load_dwordx4 v[120:123], %[f0], %[rs], 0 offen sc1\n\tbuffer_load_dwordx4 v[124:127], %[f1], %[rs], 0 offen sc1\n\t"
      "s_waitcnt vmcnt(0)\n\tv_xor_b32 %[t], %[t0], v121\n\tv_bitop3_b32 %[t], %[t], %[t1], v123 bitop3:0xf6\n\tv_bitop3_b32 %[t], %[t], %[t0], v125 bitop3:0xf6\n\tv_bitop3_b32 %[t], %[t], %[t1], v127 bitop3:0xf6\n\tv_cmp_le_u32 vcc, %[lim], %[t]\n\ts_cbranch_vccnz 3%=f\n4%=:\n\t.subsection 1\n3%=:\n\ts_mov_b64 exec, vcc\n\t.endif\n\t"
      "s_mov_b32 %[cnt], %[mx]\n\t"
      "buffer_load_dwordx4 v[96:99], %[f0], %[rs], 0 offen sc1\n\tbuffer_load_dwordx4 v[100:103], %[f1], %[rs], 0 offen sc1\n\ts_sleep %[sl]\n\t"
      "buffer_load_dwordx4 v[104:107], %[f0], %[rs], 0 offen sc1\n\tbuffer_load_dwordx4 v[108:111], %[f1], %[rs], 0 offen sc1\n\ts_sleep %[sl]\n\t"
      "1%=:\n\t"
      "buffer_load_dwordx4 v[112:115], %[f0], %[rs], 0 offen sc1\n\tbuffer_load_dwordx4 v[116:119], %[f1], %[rs], 0 offen sc1\n\t"
      "s_waitcnt vmcnt(4)\n\tv_xor_b32 %[t], %[t0], v97\n\tv_bitop3_b32 %[t], %[t], %[t1], v99 bitop3:0xf6\n\tv_bitop3_b32 %[t], %[t], %[t0], v101 bitop3:0xf6\n\tv_bitop3_b32 %[t], %[t], %[t1], v103 bitop3:0xf6\n\tv_cmp_le_u32 vcc, %[lim], %[t]\n\tv_mov_b64 v[120:121], v[96:97]\n\tv_mov_b64 v[122:123], v[98:99]\n\tv_mov_b64 v[124:125], v[100:101]\n\tv_mov_b64 v[126:127], v[102:103]\n\ts_mov_b64 exec, vcc\n\ts_cbranch_execz 2%=f\n\t"
      "buffer_load_dwordx4 v[96:99], %[f0], %[rs], 0 offen sc1\n\tbuffer_load_dwordx4 v[100:103], %[f1], %[rs], 0 offen sc1\n\t"
      "s_waitcnt vmcnt(4)\n\tv_xor_b32 %[t], %[t0], v105\n\tv_bitop3_b32 %[t], %[t], %[t1], v107 bitop3:0xf6\n\tv_bitop3_b32 %[t], %[t], %[t0], v109 bitop3:0xf6\n\tv_bitop3_b32 %[t], %[t], %[t1], v111 bitop3:0xf6\n\tv_cmp_le_u32 vcc, %[lim], %[t]\n\tv_mov_b64 v[120:121], v[104:105]\n\tv_mov_b64 v[122:123], v[106:107]\n\tv_mov_b64 v[124:125], v[108:109]\n\tv_mov_b64 v[126:127], v[110:111]\n\ts_mov_b64 exec, vcc\n\ts_cbranch_execz 2%=f\n\t"
      "buffer_load_dwordx4 v[104:107], %[f0], %[rs], 0 offen sc1\n\tbuffer_load_dwordx4 v[108:111], %[f1], %[rs], 0 offen sc1\n\t"
      "s_waitcnt vmcnt(4)\n\tv_xor_b32 %[t], %[t0], v113\n\tv_bitop3_b32 %[t], %[t], %[t1], v115 bitop3:0xf6\n\tv_bitop3_b32 %[t], %[t], %[t0], v117 bitop3:0xf6\n\tv_bitop3_b32 %[t], %[t], %[t1], v119 bitop3:0xf6\n\tv_cmp_le_u32 vcc, %[lim], %[t]\n\tv_mov_b64 v[120:121], v[112:113]\n\tv_mov_b64 v[122:123], v[114:115]\n\tv_mov_b64 v[124:125], v[116:117]\n\tv_mov_b64 v[126:127], v[118:119]\n\ts_mov_b64 exec, vcc\n\ts_cbranch_execz 2%=f\n\t"
      "s_sub_u32 %[cnt], %[cnt], 1\n\ts_cmp_lg_u32 %[cnt], 0\n\ts_cbranch_scc1 1%=b\n2%=:\n\ts_mov_b64 exec, -1\n\t"
      ".if %[sf]\n\ts_branch 4%=b\n\t.subsection 0\n\t.endif"
      : "=&{v[120:123]}"(r[0]), "=&{v[124:127]}"(r[1]),
        "=&{v[96:99]}"(p[0]), "=&{v[100:103]}"(p[1]), "=&{v[104:107]}"(p[2]), "=&{v[108:111]}"(p[3]), "=&{v[112:115]}"(p[4]), "=&{v[116:119]}"(p[5]),
        [t] "=&v"(t), [cnt] "=&s"(cnt)
      : [f0] "v"(f[0]), [f1] "v"(f[1]), [rs] "s"(rs), [t0] "s"(tag.t0), [t1] "s"(tag.t1), [lim] "s"(4096u), [mx] "n"(SPIN_MAX), [sl] "n"(SL), [sf] "n"(SF)
      : "vcc", "scc", "memory");
}
__device__ __forceinline__ void poll2_keep(u32x4 (&p)[6]) { asm volatile("" :: "{v[96:99]}"(p[0]), "{v[100:103]}"(p[1]), "{v[104:107]}"(p[2]), "{v[108:111]}"(p[3]), "{v[112:115]}"(p[4]), "{v[116:119]}"(p[5])); }
template <int SL, int SF> __device__ __forceinline__ void poll5(u32x4 (&r)[5], u32x4 (&p)[15], const int (&f)[5], rsrc_t rs, Tag tag) {
  unsigned t, cnt;
  asm volatile(
      ".if %[sf]\n\t"
      "buffer_load_dwordx4 v[108:111], %[f0], %[rs], 0 offen sc1\n\tbuffer_load_dwordx4 v[112:115], %[f1], %[rs], 0 offen sc1\n\tbuffer_load_dwordx4 v[116:119], %[f2], %[rs], 0 offen sc1\n\tbuffer_load_dwordx4 v[120:123], %[f3], %[rs], 0 offen sc1\n\tbuffer_load_dwordx4 v[124:127], %[f4], %[rs], 0 offen sc1\n\t"
      "s_waitcnt vmcnt(0)\n\tv_xor_b32 %[t], %[t0], v109\n\tv_bitop3_b32 %[t], %[t], %[t1], v111 bitop3:0xf6\n\tv_bitop3_b32 %[t], %[t], %[t0], v113 bitop3:0xf6\n\tv_bitop3_b32 %[t], %[t], %[t1], v115 bitop3:0xf6\n\tv_bitop3_b32 %[t], %[t], %[t0], v117 bitop3:0xf6\n\tv_bitop3_b32 %[t], %[t], %[t1], v119 bitop3:0xf6\n\tv_bitop3_b32 %[t], %[t], %[t0], v121 bitop3:0xf6\n\tv_bitop3_b32 %[t], %[t], %[t1], v123 bitop3:0xf6\n\tv_bitop3_b32 %[t], %[t], %[t0], v125 bitop3:0xf6\n\tv_bitop3_b32 %[t], %[t], %[t1], v127 bitop3:0xf6\n\tv_cmp_le_u32 vcc, %[lim], %[t]\n\ts_cbranch_vccnz 3%=f\n4%=:\n\t.subsection 1\n3%=:\n\ts_mov_b64 exec, vcc\n\t.endif\n\t"
      "s_mov_b32 %[cnt], %[mx]\n\t"
      "buffer_load_dwordx4 v[48:51], %[f0], %[rs], 0 offen sc1\n\tbuffer_load_dwordx4 v[52:55], %[f1], %[rs], 0 offen sc1\n\tbuffer_load_dwordx4 v[56:59], %[f2], %[rs], 0 offen sc1\n\tbuffer_load_dwordx4 v[60:63], %[f3], %[rs], 0 offen sc1\n\tbuffer_load_dwordx4 v[64:67], %[f4], %[rs], 0 offen sc1\n\ts_sleep %[sl]\n\t"
      "buffer_load_dwordx4 v[68:71], %[f0], %[rs], 0 offen sc1\n\tbuffer_load_dwordx4 v[72:75], %[f1], %[rs], 0 offen sc1\n\tbuffer_load_dwordx4 v[76:79], %[f2], %[rs], 0 offen sc1\n\tbuffer_load_dwordx4 v[80:83], %[f3], %[rs], 0 offen sc1\n\tbuffer_load_dwordx4 v[84:87], %[f4], %[rs], 0 offen sc1\n\ts_sleep %[sl]\n\t"
      "1%=:\n\t"
      "buffer_load_dwordx4 v[88:91], %[f0], %[rs], 0 offen sc1\n\tbuffer_load_dwordx4 v[92:95], %[f1], %[rs], 0 offen sc1\n\tbuffer_load_dwordx4 v[96:99], %[f2], %[rs], 0 offen sc1\n\tbuffer_load_dwordx4 v[100:103], %[f3], %[rs], 0 offen sc1\n\tbuffer_load_dwordx4 v[104:107], %[f4], %[rs], 0 offen sc1\n\t"
      "s_waitcnt vmcnt(10)\n\tv_xor_b32 %[t], %[t0], v49\n\tv_bitop3_b32 %[t], %[t], %[t1], v51 bitop3:0xf6\n\tv_bitop3_b32 %[t], %[t], %[t0], v53 bitop3:0xf6\n\tv_bitop3_b32 %[t], %[t], %[t1], v55 bitop3:0xf6\n\tv_bitop3_b32 %[t], %[t], %[t0], v57 bitop3:0xf6\n\tv_bitop3_b32 %[t], %[t], %[t1], v59 bitop3:0xf6\n\tv_bitop3_b32 %[t], %[t], %[t0], v61 bitop3:0xf6\n\tv_bitop3_b32 %[t], %[t], %[t1], v63 bitop3:0xf6\n\tv_bitop3_b32 %[t], %[t], %[t0], v65 bitop3:0xf6\n\tv_bitop3_b32 %[t], %[t], %[t1], v67 bitop3:0xf6\n\tv_cmp_le_u32 vcc, %[lim], %[t]\n\tv_mov_b64 v[108:109], v[48:49]\n\tv_mov_b64 v[110:111], v[50:51]\n\tv_mov_b64 v[112:113], v[52:53]\n\tv_mov_b64 v[114:115], v[54:55]\n\tv_mov_b64 v[116:117], v[56:57]\n\tv_mov_b64 v[118:119], v[58:59]\n\tv_mov_b64 v[120:121], v[60:61]\n\tv_mov_b64 v[122:123], v[62:63]\n\tv_mov_b64 v[124:125], v[64:65]\n\tv_mov_b64 v[126:127], v[66:67]\n\ts_mov_b64 exec, vcc\n\ts_cbranch_execz 2%=f\n\t"
      "buffer_load_dwordx4 v[48:51], %[f0], %[rs], 0 offen sc1\n\tbuffer_load_dwordx4 v[52:55], %[f1], %[rs], 0 offen sc1\n\tbuffer_load_dwordx4 v[56:59], %[f2], %[rs], 0 offen sc1\n\tbuffer_load_dwordx4 v[60:63], %[f3], %[rs], 0 offen sc1\n\tbuffer_load_dwordx4 v[64:67], %[f4], %[rs], 0 offen sc1\n\t"
      "s_waitcnt vmcnt(10)\n\tv_xor_b32 %[t], %[t0], v69\n\tv_bitop3_b32 %[t], %[t], %[t1], v71 bitop3:0xf6\n\tv_bitop3_b32 %[t], %[t], %[t0], v73 bitop3:0xf6\n\tv_bitop3_b32 %[t], %[t], %[t1], v75 bitop3:0xf6\n\tv_bitop3_b32 %[t], %[t], %[t0], v77 bitop3:0xf6\n\tv_bitop3_b32 %[t], %[t], %[t1], v79 bitop3:0xf6\n\tv_bitop3_b32 %[t], %[t], %[t0], v81 bitop3:0xf6\n\tv_bitop3_b32 %[t], %[t], %[t1], v83 bitop3:0xf6\n\tv_bitop3_b32 %[t], %[t], %[t0], v85 bitop3:0xf6\n\tv_bitop3_b32 %[t], %[t], %[t1], v87 bitop3:0xf6\n\tv_cmp_le_u32 vcc, %[lim], %[t]\n\tv_mov_b64 v[108:109], v[68:69]\n\tv_mov_b64 v[110:111], v[70:71]\n\tv_mov_b64 v[112:113], v[72:73]\n\tv_mov_b64 v[114:115], v[74:75]\n\tv_mov_b64 v[116:117], v[76:77]\n\tv_mov_b64 v[118:119], v[78:79]\n\tv_mov_b64 v[120:121], v[80:81]\n\tv_mov_b64 v[122:123], v[82:83]\n\tv_mov_b64 v[124:125], v[84:85]\n\tv_mov_b64 v[126:127], v[86:87]\n\ts_mov_b64 exec, vcc\n\ts_cbranch_execz 2%=f\n\t"
      "buffer_load_dwordx4 v[68:71], %[f0], %[rs], 0 offen sc1\n\tbuffer_load_dwordx4 v[72:75], %[f1], %[rs], 0 offen sc1\n\tbuffer_load_dwordx4 v[76:79], %[f2], %[rs], 0 offen sc1\n\tbuffer_load_dwordx4 v[80:83], %[f3], %[rs], 0 offen sc1\n\tbuffer_load_dwordx4 v[84:87], %[f4], %[rs], 0 offen sc1\n\t"
      "s_waitcnt vmcnt(10)\n\tv_xor_b32 %[t], %[t0], v89\n\tv_bitop3_b32 %[t], %[t], %[t1], v91 bitop3:0xf6\n\tv_bitop3_b32 %[t], %[t], %[t0], v93 bitop3:0xf6\n\tv_bitop3_b32 %[t], %[t], %[t1], v95 bitop3:0xf6\n\tv_bitop3_b32 %[t], %[t], %[t0], v97 bitop3:0xf6\n\tv_bitop3_b32 %[t], %[t], %[t1], v99 bitop3:0xf6\n\tv_bitop3_b32 %[t], %[t], %[t0], v101 bitop3:0xf6\n\tv_bitop3_b32 %[t], %[t], %[t1], v103 bitop3:0xf6\n\tv_bitop3_b32 %[t], %[t], %[t0], v105 bitop3:0xf6\n\tv_bitop3_b32 %[t], %[t], %[t1], v107 bitop3:0xf6\n\tv_cmp_le_u32 vcc, %[lim], %[t]\n\tv_mov_b64 v[108:109], v[88:89]\n\tv_mov_b64 v[110:111], v[90:91]\n\tv_mov_b64 v[112:113], v[92:93]\n\tv_mov_b64 v[114:115], v[94:95]\n\tv_mov_b64 v[116:117], v[96:97]\n\tv_mov_b64 v[118:119], v[98:99]\n\tv_mov_b64 v[120:121], v[100:101]\n\tv_mov_b64 v[122:123], v[102:103]\n\tv_mov_b64 v[124:125], v[104:105]\n\tv_mov_b64 v[126:127], v[106:107]\n\ts_mov_b64 exec, vcc\n\ts_cbranch_execz 2%=f\n\t"
      "s_sub_u32 %[cnt], %[cnt], 1\n\ts_cmp_lg_u32 %[cnt], 0\n\ts_cbranch_scc1 1%=b\n2%=:\n\ts_mov_b64 exec, -1\n\t"
      ".if %[sf]\n\ts_branch 4%=b\n\t.subsection 0\n\t.endif"
      : "=&{v[108:111]}"(r[0]), "=&{v[112:115]}"(r[1]), "=&{v[116:119]}"(r[2]), "=&{v[120:123]}"(r[3]), "=&{v[124:127]}"(r[4]),
        "=&{v[48:51]}"(p[0]), "=&{v[52:55]}"(p[1]), "=&{v[56:59]}"(p[2]), "=&{v[60:63]}"(p[3]), "=&{v[64:67]}"(p[4]), "=&{v[68:71]}"(p[5]), "=&{v[72:75]}"(p[6]), "=&{v[76:79]}"(p[7]), "=&{v[80:83]}"(p[8]), "=&{v[84:87]}"(p[9]), "=&{v[88:91]}"(p[10]), "=&{v[92:95]}"(p[11]), "=&{v[96:99]}"(p[12]), "=&{v[100:103]}"(p[13]), "=&{v[104:107]}"(p[14]),
        [t] "=&v"(t), [cnt] "=&s"(cnt)
      : [f0] "v"(f[0]), [f1] "v"(f[1]), [f2] "v"(f[2]), [f3] "v"(f[3]), [f4] "v"(f[4]), [rs] "s"(rs), [t0] "s"(tag.t0), [t1] "s"(tag.t1), [lim] "s"(4096u), [mx] "n"(SPIN_MAX), [sl] "n"(SL), [sf] "n"(SF)
      : "vcc", "scc", "memory");
}
__device__ __forceinline__ void poll5_keep(u32x4 (&p)[15]) { asm volatile("" :: "{v[48:51]}"(p[0]), "{v[52:55]}"(p[1]), "{v[56:59]}"(p[2]), "{v[60:63]}"(p[3]), "{v[64:67]}"(p[4]), "{v[68:71]}"(p[5]), "{v[72:75]}"(p[6]), "{v[76:79]}"(p[7]), "{v[80:83]}"(p[8]), "{v[84:87]}"(p[9]), "{v[88:91]}"(p[10]), "{v[92:95]}"(p[11]), "{v[96:99]}"(p[12]), "{v[100:103]}"(p[13]), "{v[104:107]}"(p[14])); }
#define LDSWAITV(fs_, word, n, slp) do { unsigned fv_; \
    asm volatile("1%=:\n\tds_read_b32 %0, %2\n\ts_waitcnt lgkmcnt(0)\n\tv_readfirstlane_b32 %1, %0\n\ts_cmp_lt_u32 %1, %3\n\ts_cbranch_scc0 2%=f\n\ts_sleep %4\n\ts_branch 1%=b\n2%=:" \
                 : "=&v"(fv_), "=&s"(fs_) : "v"((unsigned)(uintptr_t)&(word)), "n"(n), "n"(slp) : "scc", "memory"); } while (0)
#define LDSINC(word) asm volatile("s_mov_b64 exec, 1\n\tds_add_u32 %0, %1\n\ts_mov_b64 exec, -1" :: "v"((unsigned)(uintptr_t)&(word)), "v"(1u) : "memory")
#define LDSWAIT(word, n, slp) do { unsigned fs0_; LDSWAITV(fs0_, word, n, slp); } while (0)

constexpr int ASTR = 104;

__global__ void __launch_bounds__(768, 3)
fused_k(const bf16*, const bf16*, const bf16*, const bf16*, const bf16*, const bf16*, const bf16*, const bf16*, unsigned char*, bf16*) {
  u32x16 ka; u32x8 kb;
  asm volatile("s_load_dwordx16 s[64:79], %2, 0x0\n\ts_load_dwordx4 s[80:83], %2, 0x40\n\ts_memrealtime s[84:85]"
               : "=&{s[64:79]}"(ka), "=&{s[80:87]}"(kb) : "s"(__builtin_amdgcn_kernarg_segment_ptr()));
  SB();
  asm volatile("s_load_dwordx2 s[86:87], %1, %2" : "+{s[80:87]}"(kb)
               : "s"(__builtin_amdgcn_kernarg_segment_ptr()), "s"((unsigned)(0x1197444480653333ull >> ((blockIdx.x >> 4) * 4)) % 16 * 8));
#define KARG(lo, hi) ({ u64 p_; asm volatile("s_mov_b64 %0, s[" #lo ":" #hi "]" : "=s"(p_) : "{s[64:79]}"(ka), "{s[80:87]}"(kb)); p_; })
#define GPTR(T, lo, hi) ((T*)(__attribute__((address_space(1))) T*)KARG(lo, hi))
#define ARGS() asm volatile("s_waitcnt lgkmcnt(0)")
#define ARGS2() ws = GPTR(unsigned char, 80, 81); rw = mkrs(ws, WS_END)

  __shared__ __attribute__((aligned(16))) float stat[NB][2];
  __shared__ __attribute__((aligned(16))) unsigned qs[NB][DH / 2];
  __shared__ __attribute__((aligned(16))) unsigned ctl[16];
  __shared__ __attribute__((aligned(16))) unsigned ofs[32], lat[8];
  __shared__ __attribute__((aligned(16))) unsigned pl[8][8];
  __shared__ unsigned tw0;
  __shared__ __attribute__((aligned(16))) float mrg[NB][16][DH];
  __shared__ __attribute__((aligned(16))) float mlw[4][32];
  __shared__ __attribute__((aligned(16))) unsigned oA[4 * NB][ASTR];
  __shared__ __attribute__((aligned(256))) u32x4 wf[DH][16];
  float (*red)[NB][64] = reinterpret_cast<float (*)[NB][64]>(&wf[0][0]);
  u64 pc0;
  asm volatile("s_getpc_b64 %0" : "=s"(pc0));
  const int h = blockIdx.x & 31, sp = blockIdx.x >> 5;
  const int tid = threadIdx.x, lane = tid & 63, wave = __builtin_amdgcn_readfirstlane(tid >> 6);
  unsigned char* ws;
  rsrc_t rw;
  Tag tag;
  u64 epoch = 0;
  unsigned role;
  auto mkrole = [&]() { unsigned u = blockIdx.x; asm volatile("" : "+s"(u)); u = 8 * (u & 31) + (u >> 5); role = u + 1 + (u < 160 ? 155 - 10 * (u / 5) : 0); };
  auto proj = [&](auto ngc, const int cg) {
    constexpr int NG = decltype(ngc)::value;
    const int hp = (role - 1) / 5;
    const int j = lane & 15, g = lane >> 4;
    typedef __attribute__((address_space(3))) const u32x4* lpu;
    typedef __attribute__((address_space(3))) s16x4* lpt;
    int ydst = WS_Y + ((role - 1 - 5 * hp) * (NH + 8) + hp) * 2048 + cg * 256 + (j & 3) * (64 * NG) + ((j >> 3) * 4 + g) * 16;
    unsigned ard = (unsigned)(uintptr_t)&oA[role <= 160 ? j : min(j, 4)][4 * g];
    const unsigned ch = 2 * cg + ((j >> 1) & 1), wb = (unsigned)(uintptr_t)&wf[j >> 2][0] + 8 * (j & 1);
    unsigned wr0 = wb + 2048 * g + 16 * (ch ^ (j & 12) ^ ((2 * g) & 3));
    u32x4 rq = {(unsigned)(uintptr_t)ws, (unsigned)((uintptr_t)ws >> 32) & 0xffffu, (unsigned)WS_END, 0x00020000u};
    u64 ym = NG == 1 ? 0x000f000f000f000full : 0x0f0f0f0f0f0f0f0full;
    asm volatile("" : "+v"(ydst), "+v"(ard), "+v"(wr0)); asm volatile("" : "+s"(rq)); asm volatile("" : "+s"(ym));
    if (PRJ) __builtin_amdgcn_s_setprio(NG);
    auto trd = [](unsigned a) { return __builtin_bit_cast(u32x2, __builtin_amdgcn_ds_read_tr16_b64_v4i16(reinterpret_cast<lpt>(a))); };
    auto frag = [&](int n, int ks) { const u32x2 lo = trd((wr0 ^ (32 * n)) + 8192 * ks), hi = trd((wr0 ^ (32 * n + 16)) + 8192 * ks + 1024);
                                     return u32x4{lo[0], lo[1], hi[0], hi[1]}; };
    {
      u32x4 bw[NG][6], af[6];
      f32x4 y[NG];
      if (NG == 1) {
        __builtin_amdgcn_s_waitcnt(0x0f70);
        FENCE();
        LDSINC(ctl[6]);
      }
      LDSWAIT(ctl[6], 4, 0);
#pragma unroll
      for (int n = 0; n < NG; ++n) {
#pragma unroll
        for (int ks = 0; ks < 6; ++ks) bw[n][ks] = frag(n, ks);
        y[n] = f32x4{0.f, 0.f, 0.f, 0.f};
      }
#pragma unroll
      for (int n = 0; n < NG; ++n) {
#pragma unroll
        for (int ks = 0; ks < 6; ++ks) asm volatile("" : "+v"(bw[n][ks]));
      }
      if (role <= 160) BAR();
#pragma unroll
      for (int ks = 0; ks < 6; ++ks) { af[ks] = *reinterpret_cast<lpu>(ard + 64 * ks); }
      SB();
#pragma unroll
      for (int ks = 0; ks < 6; ++ks)
#pragma unroll
        for (int n = 0; n < NG; ++n) y[n] = mfma(bw[n][ks], af[ks], y[n]);
      f32x4 z;
      if (NG == 1) {
#pragma unroll
        for (int r = 0; r < 4; ++r) z[r] = y[0][r] + dpp<XOR8>(y[0][r]);
      } else {
#define A8(d, s, m) "v_add_f32_dpp %" #d ", %" #s ", %" #s " row_ror:8 row_mask:0xf bank_mask:" #m "\n\t"
        asm volatile("s_nop 6\n\t" A8(0, 4, 0x3) A8(1, 5, 0x3) A8(2, 6, 0x3) A8(3, 7, 0x3) A8(0, 8, 0xc) A8(1, 9, 0xc) A8(2, 10, 0xc) A8(3, 11, 0xc)
                     : "=&v"(z[0]), "=&v"(z[1]), "=&v"(z[2]), "=&v"(z[3])
                     : "v"(y[0][0]), "v"(y[0][1]), "v"(y[0][2]), "v"(y[0][3]), "v"(y[NG - 1][0]), "v"(y[NG - 1][1]), "v"(y[NG - 1][2]), "v"(y[NG - 1][3]));
      }
#pragma unroll
      for (int r = 0; r < 4; ++r) z[r] += dpp<0x104>(z[r]);
      const u32x4 yp = g_pack(z[0], z[1], z[2], z[3], tag);
      asm volatile("s_mov_b64 exec, %3\n\tbuffer_store_dwordx4 %0, %1, %2, 0 offen sc1\n\ts_mov_b64 exec, -1\n\ts_nop 0"
                   :: "v"(yp), "v"(ydst), "s"(rq), "s"(ym) : "memory");
      asm volatile("" :: "v"(y[0]), "v"(y[NG - 1]));
    }
  };
  if (__builtin_expect(wave >= 4, 1)) {
    const int w = wave - 4, kk = lane >> 3, sub = lane & 7;
    unsigned wbase = (h * SK + (SK - WIN)) * DH;
    int voff = ((sp * 8 + w) * 8 + kk) * (DH * 2) + 16 * sub;
    asm volatile("" : "+v"(voff));
    asm volatile("" : "+s"(wbase));
    u64 tz;
    asm volatile("" : "={s[88:89]}"(tz));
    if (w == 1 && sp < 7) {
      const int ix = blockIdx.x >> 3;
      const unsigned to = (ix & 8) == 0 ? (ix & 7) * 983040 : (0x2008a00u >> ix) & 1 ? DM * FF * 2 - 64 : 128;
      asm volatile("s_waitcnt lgkmcnt(0)\n\ts_load_dword s88, s[86:87], %0" :: "s"(to), "{s[80:87]}"(kb), "{s[88:89]}"(tz) : "memory");
    } else ARGS();
    const rsrc_t rk = mkrs(GPTR(const bf16, 66, 67) + wbase, 3 * BSTR + WIN * DH * 2);
    ARGS2();
    const rsrc_t rv = mkrs(GPTR(const bf16, 68, 69) + wbase, 3 * BSTR + WIN * DH * 2);
    u32x4 tl[NBUF][3];
    unsigned tstart;
    unsigned late = 0;
    const unsigned ofa = (unsigned)(uintptr_t)&ofs[0], laa = (unsigned)(uintptr_t)&lat[w];
#define PACE(t) do { { \
      if (NCK && (t) >= CK0 - 1) { unsigned tv_; \
        asm volatile("1%=:\n\tds_read_b32 %1, %4 offset:%5\n\ts_memrealtime s[90:91]\n\ts_waitcnt lgkmcnt(0)\n\tv_readfirstlane_b32 s91, %1\n\ts_sub_u32 %0, s90, %2\n\t" \
                     "s_addk_i32 %0, %3\n\ts_sub_u32 %0, %0, s91\n\ts_cmp_lt_i32 %0, 0\n\ts_cbranch_scc0 2%=f\n\ts_sleep 1\n\ts_branch 1%=b\n2%=:\n\t" \
                     "v_mov_b32 %1, %0\n\ts_mov_b64 exec, 1\n\tds_write_b32 %6, %1\n\ts_mov_b64 exec, -1" \
                     : "=&s"(late), "=&v"(tv_) : "s"(tstart), "n"(-sched(t)), "v"(ofa), "n"(4 * (t)), "v"(laa) : "scc", "s90", "s91", "memory"); \
      } else \
      asm volatile("1%=:\n\ts_memrealtime s[90:91]\n\ts_waitcnt lgkmcnt(0)\n\ts_sub_u32 %0, s90, %1\n\ts_addk_i32 %0, %2\n\ts_cmp_lt_i32 %0, 0\n\ts_cbranch_scc0 2%=f\n\ts_sleep 1\n\ts_branch 1%=b\n2%=:" \
                   : "=&s"(late) : "s"(tstart), "n"(-sched(t)) : "scc", "s90", "s91", "memory"); \
      if (PR1) asm volatile("s_setprio 0\n\ts_cmpk_lt_u32 %0, %1\n\ts_cbranch_scc1 1%=f\n\ts_setprio 1\n\ts_cmpk_lt_u32 %0, %2\n\ts_cbranch_scc1 1%=f\n\t" \
                   "s_setprio 2\n\ts_cmpk_lt_u32 %0, %3\n\ts_cbranch_scc1 1%=f\n\ts_setprio 3\n1%=:" \
                   :: "s"(late), "n"(PR1), "n"(PR2), "n"(PR3) : "scc", "memory"); } } while (0)
#define WINDOW(t) do { if (LATE && (t) < TSW) \
      asm volatile("s_cmpk_lt_u32 %0, %1\n\ts_cbranch_scc0 1%=f\n\ts_waitcnt vmcnt(%2)\n1%=:\n\ts_waitcnt vmcnt(%3)" \
                   :: "s"(late), "n"(LATE), "n"(WINL - 1), "n"(WINB - 1) : "scc", "memory"); \
      else VMW(((t) < TSW ? WINL : WINV) - 1); } while (0)
#define ISSUE_T(t) do { PACE(t); _Pragma("unroll") for (int i_ = 0; i_ < 3; ++i_) { \
      WINDOW(t); \
      tl[(t) % NBUF][i_] = ldb_nt((t) < NT ? rk : rv, voff + 128 * i_, bat(t) * BSTR + ((t) & 3) * TSTR); \
      SB(); } } while (0)
    SB();
    if (w == 0) { if (lane < 14) ctl[2 + lane] = 0u; if (lane < 32) ofs[lane] = 0u; else lat[lane & 7] = 0u; asm volatile("s_waitcnt lgkmcnt(0)" ::: "memory"); }
    asm volatile("s_buffer_load_dword s89, %0, %1" :: "s"(sp < 4 ? rk : rv), "s"((sp & 3) * BSTR + w * 112344), "{s[88:89]}"(tz) : "memory");
    asm volatile("s_barrier" ::: "memory");
    {
      const unsigned tb = (unsigned)__builtin_amdgcn_s_memrealtime();
      tstart = __builtin_amdgcn_readfirstlane(tw0);
      tstart += TOFS + ((h * SPAN) >> 5) + ((max((int)(tb - tstart) - OB, 0) * OK8) >> 3);
      asm volatile("s_waitcnt lgkmcnt(0)" :: "{s[88:89]}"(tz), "s"(tstart) : "memory");
    }
#pragma unroll
    for (int t = 0; t < NBUF; ++t) ISSUE_T(t);
    SB();
    LDSWAIT(ctl[13], 1, 1);
    tstart = __builtin_amdgcn_readfirstlane(ctl[15]) + TOFS + ((h * SPAN) >> 5);
    u32x4 qp[3];
    float sc[NT], ls[NB], ms = 0.f;
#pragma unroll
    for (int t = 0; t < NT; ++t) {
      SB();
      if ((t & 3) == 0) {
#pragma unroll
        for (int i = 0; i < 3; ++i) qp[i] = *reinterpret_cast<const u32x4*>(&qs[bat(t)][32 * i + 4 * sub]);
      }
      float d0 = 0.f, d1 = 0.f;
#pragma unroll
      for (int i = 0; i < 3; ++i) {
        d0 = dot2(tl[t % NBUF][i][0], qp[i][0], d0); d1 = dot2(tl[t % NBUF][i][1], qp[i][1], d1);
        d0 = dot2(tl[t % NBUF][i][2], qp[i][2], d0); d1 = dot2(tl[t % NBUF][i][3], qp[i][3], d1);
      }
      asm volatile("" : "+v"(d0), "+v"(d1));
      SB();
      ISSUE_T(t + NBUF);
      SB();
      float d = d0 + d1;
      d += dpp<XOR1>(d); d += dpp<XOR2>(d); d += dpp<XOR7>(d);
      sc[t] = rbf(rbf(d) * SCALE);
      if ((t & 3) == 3) {
        float mm = fmaxf(fmaxf(sc[t - 3], sc[t - 2]), fmaxf(sc[t - 1], sc[t]));
        mm = fmaxf(mm, dpp<XOR8>(mm)); mm = x16max(mm); mm = x32max(mm);
        mm *= LOG2E;
#pragma unroll
        for (int u = t - 3; u <= t; ++u) sc[u] = __builtin_amdgcn_exp2f(fmaf(sc[u], LOG2E, -mm));
        ls[bat(t)] = (sc[t - 3] + sc[t - 2]) + (sc[t - 1] + sc[t]);
        ms = kk == bat(t) ? mm : ms;
      }
    }
    {
      const float l = x32sum(tr16(tr8(ls[0], ls[1], lane & 8), tr8(ls[2], ls[3], lane & 8)));
      asm volatile("s_mov_b64 exec, 0x01010101\n\tds_write2_b32 %0, %1, %2 offset1:32\n\ts_mov_b64 exec, -1"
                   :: "v"((unsigned)(uintptr_t)&mlw[0][(lane & 24) + w]), "v"(ms), "v"(l) : "memory");
    }
    LDSINC(ctl[4]);
    mkrole();
    u32x4 msk;
#pragma unroll
    for (int r = 0; r < 4; ++r) msk[r] = (sub >> 1) == r ? ((sub & 1) ? 0xffff0000u : 0x0000ffffu) : 0u;
    f32x4 acc[3];
    unsigned mwr = (unsigned)(uintptr_t)&mrg[0][2 * w + (lane >> 5)][32 * ((lane >> 4) & 1) + 4 * sub];
    unsigned mrd = (unsigned)(uintptr_t)&mrg[0][0][4 * (lane < 48 ? lane : 0)];
    int opub = WS_O + (h * NB * 8 + sp) * (DH * 2) + 16 * ((lane >> 4) * 8 + sub);
#pragma unroll
    for (int i = 0; i < 3; ++i) acc[i] = f32x4{0.f, 0.f, 0.f, 0.f};
    tag.t0 = __builtin_amdgcn_readfirstlane(ctl[0]); tag.t1 = __builtin_amdgcn_readfirstlane(ctl[1]);
    asm volatile("" : "+v"(mwr), "+v"(mrd), "+v"(opub));
    constexpr int BL = bat(31), TL = vdone(31);
    auto vstep = [&](int t) {
      const unsigned pd = cvtpk(sc[kpos(bat(t + NT)) + (t & 3)], sc[kpos(bat(t + NT)) + (t & 3)]);
      const u32x4 af = {pd & msk[0], pd & msk[1], pd & msk[2], pd & msk[3]};
#pragma unroll
      for (int i = 0; i < 3; ++i) acc[i] = mfma(af, tl[(t + NT) % NBUF][i], acc[i]);
    };
#define DEPOSIT(b) asm volatile("s_mov_b64 exec, %[vm]\n\ts_nop 7\n\tds_write_b128 %[a], %[x0] offset:%[o0]\n\tds_write_b128 %[a], %[x1] offset:%[o1]\n\tds_write_b128 %[a], %[x2] offset:%[o2]\n\t" \
                     "s_mov_b64 exec, -1" :: [vm] "s"(0xff00ff0000ff00ffull), [a] "v"(mwr), [x0] "v"(acc[0]), [x1] "v"(acc[1]), [x2] "v"(acc[2]), \
                        [o0] "n"((b) * 12288), [o1] "n"((b) * 12288 + 256), [o2] "n"((b) * 12288 + 512) : "memory")
#pragma unroll
    for (int t = 0; t < TL; ++t) {
      SB();
      if (t == 8) {
        LDSWAIT(ctl[12], 1, 1);
        const float fl = mlw[3][8 * BL + w];
#pragma unroll
        for (int u = 0; u < 4; ++u) sc[kpos(BL) + u] *= fl;
        pl[kk][w] = cvtpk(sc[kpos(BL) + 3], sc[kpos(BL) + 3]);
        SB();
      }
      vstep(t);
      SB();
      if ((t & 3) == 3) {
        DEPOSIT(bat(t + NT));
        if (t == 11) LDSINC(ctl[10]);
#pragma unroll
        for (int i = 0; i < 3; ++i) acc[i] = f32x4{0.f, 0.f, 0.f, 0.f};
      }
#pragma unroll
      for (int u = NT + NBUF; u < 2 * NT - 1; ++u)
        if (vdone(u) == t + 1) {
          ISSUE_T(u);
        }
      SB();
    }
    static_assert(TL > 11 && TL <= 15 && vdone(30) <= TL, "");
    typedef __attribute__((address_space(3))) const f32x4* lp4;
    u32x4 og = {0u, 0u, 0u, 0u};
    u64 om = w < 3 ? 0xffull : w < 6 ? 0x00ff00ff00ffull : 0ull;
    unsigned c5 = (unsigned)(uintptr_t)&ctl[5], i5 = w < 3;
    u32x4 rq = {(unsigned)(uintptr_t)ws, (unsigned)((uintptr_t)ws >> 32) & 0xffffu, (unsigned)WS_END, 0x00020000u};
    asm volatile("" : "+s"(om)); asm volatile("" : "+s"(rq)); asm volatile("" : "+v"(c5), "+v"(i5));
    if (w < 3) {
      u32x4 lt[8];
      PACE(31);
#pragma unroll
      for (int i = 0; i < 8; ++i) { WINDOW(31); lt[i] = ldb_nt(rv, voff, BL * BSTR + 3 * TSTR + 8 * DH * 2 * i - w * (8 * DH * 2 - 128)); SB(); }
      LDSINC(ctl[8]);
#pragma unroll
      for (int t = TL; t < NT - 1; ++t) { SB(); vstep(t); }
      SB();
      DEPOSIT(BL);
      LDSINC(ctl[14]);
      mrd = mwr + BL * (16 * DH * 4) - w * (2 * DH * 4 - 256);
      opub = WS_O + ((h * NB + BL) * 8 + sp) * (DH * 2) + 16 * (8 * w + sub);
      LDSWAIT(ctl[14], 8, 1);
      const u32x4 pa = *reinterpret_cast<const u32x4*>(&pl[kk][0]), pb = *reinterpret_cast<const u32x4*>(&pl[kk][4]);
      f32x4 pv[8];
#pragma unroll
      for (int i = 0; i < 8; ++i) pv[i] = *reinterpret_cast<lp4>(mrd + i * (2 * DH * 4));
      u32x4 af[8];
#pragma unroll
      for (int i = 0; i < 8; ++i) {
        const unsigned pd = (i < 4 ? pa : pb)[i & 3];
        af[i] = u32x4{pd & msk[0], pd & msk[1], pd & msk[2], pd & msk[3]};
        asm volatile("" : "+v"(af[i]));
      }
      acc[0] = (pv[0] + pv[1]) + (pv[2] + pv[3]); acc[1] = (pv[4] + pv[5]) + (pv[6] + pv[7]);
      unsigned tv = lane & 16 ? tag.t1 : tag.t0, tn = lane & 16 ? 27u : 23u, j0 = 0u, j1 = 0u, j2 = 0u;
      asm volatile("" : "+v"(acc[0]), "+v"(acc[1]), "+v"(opub), "+v"(tv), "+v"(tn), "+v"(j0), "+v"(j1), "+v"(j2));
      SB();
#pragma unroll
      for (int i = 0; i < 8; ++i) acc[i & 1] = mfma(af[i], lt[i], acc[i & 1]);
      acc[0] += acc[1];
      const auto s0 = __builtin_amdgcn_permlane32_swap(__float_as_uint(acc[0][0]), __float_as_uint(acc[0][2]), false, false);
      const auto s1 = __builtin_amdgcn_permlane32_swap(__float_as_uint(acc[0][1]), __float_as_uint(acc[0][3]), false, false);
      const float za = __uint_as_float(s0[0]) + dpp<XOR8>(__uint_as_float(s0[1])), zb = __uint_as_float(s1[0]) + dpp<XOR8>(__uint_as_float(s1[1]));
      unsigned ex = s_exp(za, zb);
      const auto e0 = __builtin_amdgcn_permlane32_swap(ex, ex, false, false);
      ex = max(e0[0], e0[1]);
      const auto e1 = __builtin_amdgcn_permlane16_swap(ex, ex, false, false);
      ex = max(e1[0], e1[1]);
      const auto s2 = __builtin_amdgcn_permlane32_swap(s_two(za, zb, ex), j0, false, false);
      const auto s3 = __builtin_amdgcn_permlane16_swap(s2[0] | (s2[1] << 20), j1, false, false);
      const auto s4 = __builtin_amdgcn_permlane16_swap((s2[1] >> 12) | (__builtin_amdgcn_ubfe(ex, tn, 4) << 8) | tv, j2, false, false);
      og = u32x4{s3[0], s4[0], s3[1], s4[1]};
    } else {
#pragma unroll
      for (int t = TL; t < NT - 1; ++t) { SB(); vstep(t); }
      SB();
      DEPOSIT(BL);
      LDSINC(ctl[14]);
      if (w < 6) {
        const unsigned pb = (VORD >> (4 * (w - 3))) & 3u;
        f32x4 pv[16];
        LDSWAIT(ctl[10], 8, 1);
        mrd += pb * (16 * DH * 4); opub += pb * (8 * DH * 2);
        const f32x4 fa = *reinterpret_cast<const f32x4*>(&mlw[3][8 * pb]), fb = *reinterpret_cast<const f32x4*>(&mlw[3][8 * pb + 4]);
#pragma unroll
        for (int i = 0; i < 16; ++i) pv[i] = *reinterpret_cast<lp4>(mrd + i * (DH * 4));
        SB();
#pragma unroll
        for (int i = 0; i < 16; i += 2) pv[i] += pv[i + 1];
#pragma unroll
        for (int i = 0; i < 16; i += 4) pv[i] = (i < 8 ? fa : fb)[(i >> 1) & 3] * pv[i] + (i < 8 ? fa : fb)[((i >> 1) & 3) + 1] * pv[i + 2];
        pv[0] = (pv[0] + pv[4]) + (pv[8] + pv[12]);
        unsigned ex = max(s_exp(pv[0][0], pv[0][1]), s_exp(pv[0][2], pv[0][3]));
        ex = max(ex, __float_as_uint(dpp<XOR8>(__uint_as_float(ex))));
        const unsigned hi = s_two(pv[0][2], pv[0][3], ex);
        og[0] = s_two(pv[0][0], pv[0][1], ex) | (hi << 20);
        og[1] = (hi >> 12) | (lane & 8 ? ((ex >> 27) << 8) | tag.t1 : (((ex >> 23) & 15u) << 8) | tag.t0);
        og[2] = __float_as_uint(dpp<XOR8>(__uint_as_float(og[0]))); og[3] = __float_as_uint(dpp<XOR8>(__uint_as_float(og[1])));
        asm volatile("" : "+v"(og), "+v"(opub));
        if (EPB) LDSWAIT(ctl[5], EPB, 1);
      }
    }
    __builtin_amdgcn_s_waitcnt(0x0f70);
    asm volatile("s_mov_b64 exec, %3\n\tbuffer_store_dwordx4 %0, %1, %2, 0 offen sc1\n\ts_mov_b64 exec, 1\n\tds_add_u32 %4, %5\n\ts_mov_b64 exec, -1"
                 :: "v"(og), "v"(opub), "s"(rq), "s"(om), "v"(c5), "v"(i5) : "memory");
    if (role <= 160 && w < 6) {
      const int hp = (role - 1) / 5;
      const int hf = w >= 3, bb = lane >> 4, pg = 2 * hf + ((lane >> 3) & 1), gq = 8 * (w - 3 * hf) + sub;
      const int f0 = WS_O + ((hp * NB + bb) * 8 + 2 * pg) * (DH * 2) + 16 * gq;
      const int f[2] = {f0, f0 + DH * 2};
      unsigned awr = (unsigned)(uintptr_t)&oA[4 * pg + bb][4 * gq];
      u32x4 r[2], p[6];
      LDSWAIT(ctl[9], 1, 1);
      f32x2 sq = *reinterpret_cast<const f32x2*>(&mlw[2][8 * bb + 2 * pg]);
      asm volatile("" : "+v"(sq), "+v"(awr));
      const unsigned tpo = tstart - ((h * SPAN) >> 5) + __builtin_amdgcn_readfirstlane(ofs[31]);
      LDSWAIT(ctl[5], 3, 1);
      if (TDL1) __builtin_amdgcn_s_sleep(TDL1);
      if (PT1) while ((unsigned)__builtin_amdgcn_s_memrealtime() - tpo < (unsigned)(sched(31) + max(PT1 + ((hp * SPAN) >> 5), PA))) __builtin_amdgcn_s_sleep(1);
      poll2<PSL1, SF1>(r, p, f, rw, tag);
      float o[8];
      s_acc(o, r[0], sq[0], true); s_acc(o, r[1], sq[1], false);
      *reinterpret_cast<__attribute__((address_space(3))) u32x4*>(awr) = u32x4{cvtpk(o[0], o[1]), cvtpk(o[2], o[3]), cvtpk(o[4], o[5]), cvtpk(o[6], o[7])};
      BAR();
      poll2_keep(p);
    }
    if (role <= 200 && w >= 6) proj(std::integral_constant<int, 2>{}, 2 * w - 8);
    if (w == 3 && role - 161u < 80u) {
      const int f = role - 161;
      int oo = ((f & 3) * DM + 32 * (f >> 2) + 4 * (lane & 7)) * 2;
      const unsigned xv = __builtin_amdgcn_raw_buffer_load_b32(mkrs(GPTR(const bf16, 64, 65), NB * DM * 2), oo + 4 * (lane >> 5), 0, 0);
      float x0 = bflo(xv), x1 = bfhi(xv);
      const int yo = WS_Y + ((f >> 4) * (NH + 8) + (lane >> 3)) * 2048 +
                     (f & 8 ? (f & 15) * 128 + (lane & 7) * 16 : (f & 4) * 128 + (lane & 4) * 64 + (f & 3) * 64 + (lane & 3) * 16);
      int fy[5] = {yo, yo + 8 * 2048, yo + 16 * 2048, yo + 24 * 2048, yo + 32 * 2048};
      const u64 po = KARG(82, 83);
      u32x4 ro = {(unsigned)po, (unsigned)(po >> 32) & 0xffffu, (unsigned)(NB * DM * 2), 0x00020000u};
      asm volatile("" : "+v"(x0), "+v"(x1), "+v"(oo), "+v"(fy[0]), "+v"(fy[1]), "+v"(fy[2]), "+v"(fy[3]), "+v"(fy[4])); asm volatile("" : "+s"(ro));
      LDSWAIT(ctl[5], 3, 4);
      if (TDL2) __builtin_amdgcn_s_sleep(TDL2);
      const unsigned tpo = tstart - ((h * SPAN) >> 5) + __builtin_amdgcn_readfirstlane(ofs[31]);
      if (PT2) while ((unsigned)__builtin_amdgcn_s_memrealtime() - tpo < (unsigned)sched(31) + PT2) __builtin_amdgcn_s_sleep(1);
      u32x4 p[15], r[5];
      poll5<PSL, SF2>(r, p, fy, rw, tag);
      const f32x4 v = (g_val(r[0]) + g_val(r[1])) + (g_val(r[2]) + g_val(r[3])) + g_val(r[4]);
      float a0 = x16sum(tr32(v[0], v[2])), a1 = x16sum(tr32(v[1], v[3]));
      a0 += dpp<XOR8>(a0); a1 += dpp<XOR8>(a1);
      const unsigned r0 = cvtpk(x0 + a0, x1 + a1);
      const auto r1 = __builtin_amdgcn_permlane32_swap(r0, r0, false, false);
      asm volatile("s_mov_b64 exec, 0xff\n\tbuffer_store_dwordx2 %0, %1, %2, 0 offen sc1\n\ts_mov_b64 exec, -1"
                   :: "v"(u32x2{r1[0], r1[1]}), "v"(oo), "s"(ro) : "memory");
      poll5_keep(p);
    }
  } else {
    const int w = wave;
    if (PRS) __builtin_amdgcn_s_setprio(PRS);
    const int j = lane & 15, g = lane >> 4;
    const bool isq = sp < 5;
    const int fu = 3 * h + sp - 5, cb = fu >> 1;
    const int qm = -(int)isq, s0 = 5 * (fu & 1) + (qm & (6 * sp - 5 * (fu & 1)));
    int ld2 = FF * 2 + (qm & (NH * DH * 2 - FF * 2));
    int vo = ((lane & 8) * 4 + (lane >> 4) * 8) * ld2 + 16 * (lane & 7);
    const int qa = ((s0 + w) * 26) >> 8, ra = s0 + w - 10 * qa;
    const int c3 = cb >= 24;
    int soa = 64 * ra * ld2 + 128 * qa + 128 * (cb - 24 * c3) + (qm & (h * DH * 2 - 128 * (cb - 24 * c3))), am = isq ? 6 : 10 + 2 * c3;
    asm volatile("" : "+v"(vo));
    asm volatile("" : "+s"(ld2)); asm volatile("" : "+s"(soa));
    u64 up;
    asm volatile("s_mov_b32 m0, %1\n\ts_waitcnt lgkmcnt(0)\n\ts_movrels_b64 %0, s[64:65]" : "=s"(up) : "s"(am), "{s[64:79]}"(ka));
    const void* um = (const void*)(__attribute__((address_space(1))) const void*)up;
    const int un = DM * ld2;
    u32x4 wv[12], xa, xr[2];
    u64 tbar;
    {
      const rsrc_t ru = mkrs(um, un);
#pragma unroll
      for (int e = 0; e < 8; ++e) {
        if (e == BPOS) { if (w == 0) tw0 = (unsigned)KARG(84, 85); BAR(); tbar = __builtin_amdgcn_s_memrealtime(); SB(); }
        wv[e] = ldb_nt(ru, vo, soa + e * ld2); SB();
      }
    }
    const int n1 = isq ? (0x62646 >> (4 * sp)) & 15 : 6;
    const int gb = s0 + 4 + (w >> 1), qb = (gb * 26) >> 8, rb = gb - 10 * qb;
    ARGS2();
    const bf16 *x = GPTR(const bf16, 64, 65), *wo = GPTR(const bf16, 72, 73), *w2 = GPTR(const bf16, 78, 79);
    const unsigned te = __builtin_amdgcn_readfirstlane(tw0);
    const unsigned tstart = te + ((max((int)((unsigned)tbar - te) - OB, 0) * OK8) >> 3);
    const rsrc_t rc = mkrs(reinterpret_cast<const void*>(pc0 & ~255ull), CODEB);
    const int cofs = 1024 * (blockIdx.x >> 3);
    {
      const rsrc_t rx = mkrs(x, NB * DM * 2);
      const int xo = ((j & 3) * DM + 32 * ((j >> 2) & 1) + 8 * g) * 2;
      {
        const rsrc_t ru = mkrs(um, isq || w < 2 ? un : 0);
#pragma unroll
        for (int e = 0; e < 4; ++e) { wv[8 + e] = ldb_nt(ru, vo, soa + (64 * (rb - ra) + 4 * (w & 1) + e) * ld2 + 128 * (qb - qa)); SB(); }
      }
      const unsigned cv = CPE ? __builtin_amdgcn_raw_buffer_load_b32(rc, 4 * tid, cofs, 0) : 0u;
#pragma unroll
      for (int i = 0; i < 2; ++i) xr[i] = ldb(rx, (w * DM + 16 * (lane < 40 ? lane : 0) + 8 * i) * 2, 0);
      xa = ldb(rx, xo, 128 * ra);
      SB();
      const u32x2 xb = __builtin_amdgcn_raw_buffer_load_b64(rx, xo, 128 * rb + 8 * (w & 1), 0);
      const u32x2 ep = __builtin_amdgcn_raw_buffer_load_b64(rw, WS_EPOCH, 0, AUX_SC1);
      SB();
      {
        float v[16]; float sm = 0.f;
#pragma unroll
        for (int i = 0; i < 2; ++i)
#pragma unroll
          for (int k = 0; k < 4; ++k) { v[8 * i + 2 * k] = bflo(xr[i][k]); v[8 * i + 2 * k + 1] = bfhi(xr[i][k]); sm += v[8 * i + 2 * k] + v[8 * i + 2 * k + 1]; }
        const float mean = wave_sum(lane < 40 ? sm : 0.f) * (1.f / DM);
        float sq = 0.f;
#pragma unroll
        for (int k = 0; k < 16; ++k) { v[k] -= mean; sq += v[k] * v[k]; }
        const float var = wave_sum(lane < 40 ? sq : 0.f) * (1.f / DM);
        stat[w][0] = rbf(mean); stat[w][1] = 1.f / rbf(sqrtf(rbf(rbf(var) + 1e-5f)));
        unsigned fv, fs;
        asm volatile("s_mov_b64 exec, 1\n\tds_add_u32 %2, %3\n\ts_mov_b64 exec, -1\n1%=:\n\tds_read_b32 %0, %2\n\ts_waitcnt lgkmcnt(0)\n\tv_readfirstlane_b32 %1, %0\n\ts_cmp_lt_u32 %1, 4\n\ts_cbranch_scc1 1%=b"
                     : "=&v"(fv), "=&s"(fs) : "v"((unsigned)(uintptr_t)&ctl[7]), "v"(lane == 0 ? 1u : 0u) : "scc", "memory");
      }
      SB();
      const float mean = stat[j & 3][0], rinv = stat[j & 3][1];
      auto lnx = [&](unsigned v) { const unsigned c = cvtpk(bflo(v) - mean, bfhi(v) - mean); return cvtpk(bflo(c) * rinv, bfhi(c) * rinv); };
#pragma unroll
      for (int st = 0; st < 2; ++st) {
        f32x4 d[8];
        if (st == 0) {
          const u32x4 af = {lnx(xa[0]), lnx(xa[1]), lnx(xa[2]), lnx(xa[3])};
#pragma unroll
          for (int ec = 0; ec < 8; ++ec) d[ec] = mfma(af, bfrag(&wv[0], ec), f32x4{0.f, 0.f, 0.f, 0.f});
        } else {
          const u32x2 af = {lnx(xb[0]), lnx(xb[1])};
#pragma unroll
          for (int ec = 0; ec < 8; ++ec) {
            const u32x2 bf = {__builtin_amdgcn_perm(wv[9][ec >> 1], wv[8][ec >> 1], (ec & 1) ? 0x07060302u : 0x05040100u),
                              __builtin_amdgcn_perm(wv[11][ec >> 1], wv[10][ec >> 1], (ec & 1) ? 0x07060302u : 0x05040100u)};
            d[ec] = __builtin_amdgcn_mfma_f32_16x16x16bf16_1k(__builtin_bit_cast(s16x4, af), __builtin_bit_cast(s16x4, bf), f32x4{0.f, 0.f, 0.f, 0.f}, 0, 0, 0);
          }
        }
        if (g == (j >> 3)) {
#pragma unroll
          for (int bb = 0; bb < NB; ++bb) {
            *reinterpret_cast<f32x4*>(&red[2 * (w + 4 * st) + g][bb][8 * (j & 7)]) = f32x4{d[0][bb], d[1][bb], d[2][bb], d[3][bb]};
            *reinterpret_cast<f32x4*>(&red[2 * (w + 4 * st) + g][bb][8 * (j & 7) + 4]) = f32x4{d[4][bb], d[5][bb], d[6][bb], d[7][bb]};
          }
        }
      }
      epoch = ((u64)__builtin_amdgcn_readfirstlane(ep[0]) | ((u64)__builtin_amdgcn_readfirstlane(ep[1]) << 32)) + 1ull;
      tag.t0 = (unsigned)epoch << 12;
      tag.t1 = ((unsigned)epoch * 0x9E377u ^ (unsigned)(epoch >> 20)) << 12;
      if (tid == 0) { ctl[0] = tag.t0; ctl[1] = tag.t1; }
      LDSINC(ctl[2]);
      asm volatile("" :: "v"(cv));
      if (w == 0) {
        LDSWAIT(ctl[2], 4, 0);
        constexpr int QS = NB * NH * DH * 4;
        const int qo = WS_Q + (g * NH * DH + h * DH + 4 * j) * 4;
        {
          f32x4 va = {0.f, 0.f, 0.f, 0.f}, vb = {0.f, 0.f, 0.f, 0.f};
#pragma unroll
          for (int p = 0; p < 8; ++p) {
            const f32x4 v = *reinterpret_cast<const f32x4*>(&red[2 * p][g][4 * j]) + *reinterpret_cast<const f32x4*>(&red[2 * p + 1][g][4 * j]);
            const f32x4 z = {0.f, 0.f, 0.f, 0.f};
            va += min(p, 4) < n1 ? v : z; vb += min(p, 4) >= n1 ? v : z;
          }
          const int dst = isq ? qo + ((0x12110 >> (4 * sp)) & 15) * QS + (sp >> 1) * 256
                              : WS_GU + (((fu & 1) * NB + g) * (2 * FF) + 64 * cb + 4 * j) * 4;
          const u32x4 pa = p_pack(va, tag);
          if (PTL) {
            unsigned due = tstart + TOFS + sched(PTL) + POF + ((h * SPAN) >> 5);
            if ((int)((unsigned)__builtin_amdgcn_s_memrealtime() - due) > PWN) due += TAU;
            while ((int)((unsigned)__builtin_amdgcn_s_memrealtime() - due) < 0) __builtin_amdgcn_s_sleep(1);
          }
          st_ag(rw, dst, pa);
          if (isq && lane < ((FL & 1) ? 8 : 1)) st_ag(rw, WS_OR + h * 1024 + 128 * sp + 16 * lane, u32x4{tstart, tag.t0, 0u, tag.t1});
          if (n1 < 6) st_ag(rw, qo + ((sp + 1) >> 1) * 256, p_pack(vb, tag));
        }
        while ((unsigned)__builtin_amdgcn_s_memrealtime() - tstart < (unsigned)(TOFS + sched(QTL) + QOF + ((h * SPAN) >> 5))) __builtin_amdgcn_s_sleep(2);
        u32x4 ga[3], gb[3], gc, gd;
#pragma unroll 1
        for (int it = 0; it < SPIN_MAX; ++it) {
          FENCE();
          bool ok = true;
#pragma unroll
          for (int i = 0; i < 3; ++i) { ga[i] = ld_ag(rw, qo + 256 * i); gb[i] = ld_ag(rw, qo + QS + 256 * i); }
          gc = ld_ag(rw, qo + 2 * QS + 256);
          gd = ld_ag(rw, WS_OR + h * 1024 + 128 * min(lane, 4));
#pragma unroll
          for (int i = 0; i < 3; ++i) ok = ok && g_ok(ga[i], tag) && g_ok(gb[i], tag);
          if (__all(ok && g_ok(gc, tag) && g_ok(gd, tag))) break;
          __builtin_amdgcn_s_sleep(QBACK);
        }
#pragma unroll
        for (int i = 0; i < 3; ++i) {
          f32x4 qa = p_val(ga[i]) + p_val(gb[i]);
          if (i == 1) qa += p_val(gc);
          *reinterpret_cast<u32x2*>(&qs[g][2 * j + 32 * i]) = u32x2{cvtpk(qa[0], qa[1]), cvtpk(qa[2], qa[3])};
        }
        {
          const unsigned o0 = __builtin_amdgcn_readfirstlane(gd[0]);
          int dm = 0;
#pragma unroll
          for (int i = 1; i < 5; ++i) dm += (int)(__builtin_amdgcn_readlane(gd[0], i) - o0);
          ctl[15] = tstart + min(max((int)(o0 - tstart) + ((dm * 13) >> 6), -100), 200);
        }
        LDSINC(ctl[13]);
      }
    }
    mkrole();
    if (role - 161u < 40u) {
      const int hp = (role - 1) / 5;
      if (w < 3) {
        const int go = WS_GU + (g * (2 * FF) + DH * (hp - NH) + 64 * w + 4 * j) * 4;
        u32x4 g0, g1, g2, g3;
        while ((unsigned)__builtin_amdgcn_s_memrealtime() - tstart < (unsigned)(TOFS + sched(HGT) + 2 * TAU2 * w + 15 + ((h * SPAN) >> 5))) __builtin_amdgcn_s_sleep(8);
#pragma unroll 1
        for (int it = 0; it < SPIN_MAX; ++it) {
          FENCE();
          g0 = ld_ag(rw, go); g1 = ld_ag(rw, go + FF * 4);
          g2 = ld_ag(rw, go + NB * 2 * FF * 4); g3 = ld_ag(rw, go + NB * 2 * FF * 4 + FF * 4);
          if (__all(g_ok(g0, tag) && g_ok(g1, tag) && g_ok(g2, tag) && g_ok(g3, tag))) break;
          __builtin_amdgcn_s_sleep(32);
        }
        const f32x4 gt = p_val(g0) + p_val(g2), up = p_val(g1) + p_val(g3);
        f32x4 hv;
#pragma unroll
        for (int i = 0; i < 4; ++i) { const float gg = rbf(gt[i]); hv[i] = rbf(gg / (1.f + __expf(-gg))) * rbf(up[i]); }
        *reinterpret_cast<u32x2*>(&oA[g][32 * w + 2 * j]) = u32x2{cvtpk(hv[0], hv[1]), cvtpk(hv[2], hv[3])};
      } else {
        if (lane < DH / 4) *reinterpret_cast<u32x2*>(&oA[4][2 * lane]) = u32x2{0u, 0u};
      }
    }
    if (NCK && w == 3) {
      int ofv = 0;
      if (PRB) { const unsigned pb = __builtin_amdgcn_raw_buffer_load_b32(rw, WS_BC + 4096 * (lane >> 4) + 512 * (h & 7) + 128 * (lane & 3), 0, AUX_SC1); asm volatile("" :: "v"(pb)); }
      const int t5 = min(lane, 31);
      const unsigned due = tstart + TOFS + ((h * SPAN) >> 5) + sched(t5);
#pragma unroll 1
      for (int k = 0; k < NCK; ++k) {
        const int c = CK0 + k * CKP;
        if (sp == RSP) {
          const unsigned t1 = __builtin_amdgcn_readlane(due + ofv, c) + 30;
          while ((int)((unsigned)__builtin_amdgcn_s_memrealtime() - t1) < 0) __builtin_amdgcn_s_sleep(2);
          FENCE();
          const u32x4 la = *reinterpret_cast<const u32x4*>(&lat[0]), lb = *reinterpret_cast<const u32x4*>(&lat[4]);
          const unsigned ls = (la[0] + la[1] + la[2] + la[3] + lb[0] + lb[1] + lb[2] + lb[3]) >> 3;
          if (ls >= LTH && lane < 8) st_ag(rw, WS_BC + 4096 * k + 512 * lane + 16 * h, u32x4{min(ls, 1000u), tag.t0, 0u, tag.t1});
        }
        const unsigned t2 = __builtin_amdgcn_readlane(due + ofv, c + CKR) + 15;
        while ((int)((unsigned)__builtin_amdgcn_s_memrealtime() - t2) < 0) __builtin_amdgcn_s_sleep(2);
        FENCE();
        const u32x4 mk = ld_ag(rw, WS_BC + 4096 * k + 512 * (h & 7) + 16 * t5);
        const int sm = (int)wave_sum(g_ok(mk, tag) && lane < 32 ? (float)mk[0] : 0.f);
        const int need = ((sm * KG) >> 9) - (__builtin_amdgcn_readlane(ofv, min(c + CKE + CKS - 1, 31)) - __builtin_amdgcn_readlane(ofv, c));
        ofv += min(max(need, 0) / CKS, DMX) * min(max(t5 - (c + CKE) + 1, 0), CKS);
        if (KI && need >= KI) ofv += max(t5 - (c + CKE + CKS) + 1, 0);
        if (lane < 32) ofs[lane] = ofv;
      }
    }
    if (w == 2) {
      LDSWAIT(ctl[4], 1, 16);
      if (CPL) { const u32x4 cw = ldb(rc, 16 * lane, cofs); asm volatile("" :: "v"(cw)); }
    }
    if (w == 1) {
      LDSWAIT(ctl[4], 8, 16);
      const float mi = mlw[0][lane & 31], li = mlw[1][lane & 31];
      float M = fmaxf(mi, dpp<XOR1>(mi)); M = fmaxf(M, dpp<XOR2>(M)); M = rnd22(fmaxf(M, dpp<XOR7>(M)));
      const float fw = __builtin_amdgcn_exp2f(mi - M);
      mlw[3][lane & 31] = fw;
      LDSINC(ctl[12]);
      float L = li * fw;
      L += dpp<XOR1>(L); L += dpp<XOR2>(L); L += dpp<XOR7>(L);
      const u32x4 gp = g_pack(M, L, dpp<XOR8>(M), dpp<XOR8>(L), tag);
      if (FL & 2) { if ((lane & 44) == 0) st_ag(rw, WS_ML + (h * 8 + sp) * 128 + (lane & 16) + 32 * (lane & 3), gp); }
      else if (lane == 0 || lane == 16) st_ag(rw, WS_ML + (h * 8 + sp) * 128 + lane, gp);
    }
    if (role <= 200) {
      const int hp = (role - 1) / 5;
      if (w == 1 && role <= 160) {
        while ((unsigned)__builtin_amdgcn_s_memrealtime() - tstart < (unsigned)sched(24) + TOFS + MLD + ofs[24]) __builtin_amdgcn_s_sleep(8);
        u32x4 g0;
#pragma unroll 1
        for (int it = 0; it < SPIN_MAX; ++it) {
          FENCE();
          g0 = ld_ag(rw, WS_ML + (hp * 8 + (lane & 7)) * 128 + (lane & 16));
          if (__all(g_ok(g0, tag))) break;
          __builtin_amdgcn_s_sleep(12);
        }
        const f32x4 a0 = g_val(g0);
        const float mq = (lane & 8) ? a0[2] : a0[0], lq = (lane & 8) ? a0[3] : a0[1];
        float Mx = fmaxf(mq, dpp<XOR1>(mq)); Mx = fmaxf(Mx, dpp<XOR2>(Mx)); Mx = fmaxf(Mx, dpp<XOR7>(Mx));
        const float e0 = __builtin_amdgcn_exp2f(mq - Mx);
        float lt = e0 * lq; lt += dpp<XOR1>(lt); lt += dpp<XOR2>(lt); lt += dpp<XOR7>(lt);
        mlw[2][lane & 31] = e0 / lt * (2.f / 511.f);
        LDSINC(ctl[9]);
      }
      LDSWAIT(ctl[8], 3, 2);
      if (role <= 160) __builtin_amdgcn_s_sleep(WD1); else __builtin_amdgcn_s_sleep(WDF);
      if (WTA) { const unsigned tw = tstart + __builtin_amdgcn_readfirstlane(ofs[31]);
        while ((unsigned)__builtin_amdgcn_s_memrealtime() - tw < (unsigned)sched(31) + TOFS + WTA) __builtin_amdgcn_s_sleep(1); }
      const rsrc_t ru = mkrs((role <= 160 ? wo : w2 - NH * DH * DM) + (size_t)(hp * DH + 48 * w) * DM, 48 * DM * 2);
      const int vo = g * (DM * 2) + 256 * (role - 1 - 5 * hp) + 16 * (j ^ (4 * g));
#pragma unroll
      for (int e = 0; e < 12; ++e) {
        if (e && e % WCH == 0 && WD2) { SB(); __builtin_amdgcn_s_sleep(WD2); SB(); }
        __builtin_amdgcn_raw_ptr_buffer_load_lds(ru, (__attribute__((address_space(3))) void*)&wf[48 * w + 4 * e][0], 16, vo ^ (16 * (e & 3)), 4 * e * (DM * 2), 0, AUX_NT);
      }
      SB();
      proj(std::integral_constant<int, 1>{}, w);
    }
    if (w == 2 && role == 256) {
      LDSWAIT(ctl[8], 3, 8);
#pragma unroll 1
      for (int it = 0; it < SPIN_MAX; ++it) {
        FENCE();
        bool ok = true;
#pragma unroll
        for (int i = 0; i < 4; ++i) ok = ok && g_ok(ld_ag(rw, WS_ML + (64 * i + lane) * 128), tag);
        if (__all(ok)) break;
        __builtin_amdgcn_s_sleep(32);
      }
      if (lane == 0) __hip_atomic_store(reinterpret_cast<u64*>(ws + WS_EPOCH), epoch, __ATOMIC_RELAXED, __HIP_MEMORY_SCOPE_AGENT);
    }
  }
}

extern "C" void kernel_launch(void* const* d_in, const int* in_sizes, int n_in, void* d_out, int out_size, void* d_ws,
                              size_t ws_size, hipStream_t stream) {
  fused_k<<<256, 768, 0, stream>>>((const bf16*)d_in[0], (const bf16*)d_in[1], (const bf16*)d_in[2], (const bf16*)d_in[3],
                                   (const bf16*)d_in[4], (const bf16*)d_in[6], (const bf16*)d_in[7], (const bf16*)d_in[8],
                                   (unsigned char*)d_ws, (bf16*)d_out);
}
```

```cpp
#include <hip/hip_runtime.h>
#include <hip/hip_bf16.h>
#include <cstdint>

using bf16 = __hip_bfloat16;
using u32x2 = __attribute__((ext_vector_type(2))) unsigned;
using u32x4 = __attribute__((ext_vector_type(4))) unsigned;
using u32x16 = __attribute__((ext_vector_type(16))) unsigned;
using u32x8 = __attribute__((ext_vector_type(8))) unsigned;
using s16x4 = __attribute__((ext_vector_type(4))) short;
using f32x2 = __attribute__((ext_vector_type(2))) float;
using f32x4 = __attribute__((ext_vector_type(4))) float;
using bf16x2 = __attribute__((ext_vector_type(2))) __bf16;
using bf16x8 = __attribute__((ext_vector_type(8))) __bf16;
typedef unsigned long long u64;

constexpr int NB = 4, DM = 640, NH = 32, DH = 192, SK = 8192, WIN = 2048, FF = 1536;
constexpr float LOG2E = 1.4426950408889634f;
constexpr float SCALE = 0.07216878364870322f;
constexpr int WINL = 4;
constexpr int WINV = 12;
constexpr int TSW = 24;
constexpr int QBACK = 16;
constexpr int TAU = 83;
constexpr int TK = 7;
constexpr int TAU2 = 80;
constexpr int PR1 = 60;
constexpr int PR2 = 150;
constexpr int PR3 = 300;
constexpr int PRS = 3;
constexpr int CPE = 1;
constexpr int CODEB = 29952;
constexpr int PT1 = 165;
constexpr int PT2 = 360;
constexpr int EPB = 1;
constexpr int SPAN = 80;
constexpr int WD2 = 12;
constexpr int TOFS = 154;
constexpr int RING = 9;
constexpr int PSL = 6;
constexpr int HGT = 12;
constexpr int LATE = 100;
constexpr int WINB = 8;
constexpr int MLD = 330;
constexpr int OB = 36;
constexpr int OK8 = 7;
constexpr int PA = 150;
constexpr int EAG = 3;
constexpr int WD1 = 68;
constexpr int QTL = 6;
constexpr int QOF = 60;
constexpr int KORD = 0x3210;
constexpr int VORD = 0x3210;
constexpr int XD = 5;
constexpr int XV = 3;
constexpr int PTL = 4;
constexpr int POF = 15;
constexpr int PWN = 25;
constexpr int XM = 2;
constexpr int NCK = 4;
constexpr int CK0 = 7;
constexpr int CKP = 5;
constexpr int CKR = 3;
constexpr int CKE = 5;
constexpr int LTH = 10;
constexpr int KG = 64;
constexpr int DMX = 8;
constexpr int RSP = 6;
constexpr int CKS = 5;
constexpr int KI = 20;
constexpr int BPOS = 5;
constexpr int WDF = 92;
constexpr int LA = 110;
constexpr int bat(int t) { return ((t < 16 ? KORD : VORD) >> (t & 12)) & 3; }
constexpr int kpos(int b) { return bat(0) == b ? 0 : bat(4) == b ? 4 : bat(8) == b ? 8 : 12; }
constexpr int V2 = bat(16) == 2 ? 16 : bat(20) == 2 ? 20 : bat(24) == 2 ? 24 : 28;
constexpr int cl4(int v) { return v < 0 ? 0 : v > 4 ? 4 : v; }
constexpr int sched(int t) { return (t <= TK ? TAU * t : TAU * TK + TAU2 * (t - TK)) + XD * cl4(t - kpos(2)) + XV * cl4(t - V2) - XM * (t > 12 ? (t > 24 ? 12 : t - 12) : 0) / 4; }
constexpr int vdone(int u) { return EAG ? (u == 25 ? 1 : u == 26 ? 3 : u == 27 ? 7 : u - 15 - EAG) : u - 24; }
constexpr int NBUF = RING;
constexpr int NT = 16;
constexpr int TSTR = 8 * 8 * 8 * DH * 2;
constexpr int BSTR = NH * SK * DH * 2;

constexpr int WS_EPOCH = 0;
constexpr int WS_Q = 256;
constexpr int WS_GU = WS_Q + 3 * NB * NH * DH * 4;
constexpr int WS_ML = WS_GU + 2 * NB * 2 * FF * 4;
constexpr int WS_OR = WS_ML + NH * 8 * 128;
constexpr int WS_BC = WS_OR + NH * 1024;
constexpr int WS_O = WS_BC + 8 * 4096;
constexpr int WS_Y = WS_O + NH * NB * 8 * DH * 2;
constexpr int WS_END = WS_Y + 5 * (NH + 8) * 128 * NB * 4;

__device__ __forceinline__ float dot2(unsigned k, unsigned q, float acc) {
  return __builtin_amdgcn_fdot2_f32_bf16(__builtin_bit_cast(bf16x2, k), __builtin_bit_cast(bf16x2, q), acc, false);
}
__device__ __forceinline__ float bflo(unsigned v) { return __builtin_bit_cast(float, v << 16); }
__device__ __forceinline__ float bfhi(unsigned v) { return __builtin_bit_cast(float, v & 0xffff0000u); }
template <int CTRL> __device__ __forceinline__ float dpp(float x) {
  return __builtin_bit_cast(float, __builtin_amdgcn_mov_dpp(__builtin_bit_cast(int, x), CTRL, 0xf, 0xf, true));
}
constexpr int XOR1 = 0xB1, XOR2 = 0x4E, XOR7 = 0x141, XOR8 = 0x128;
__device__ __forceinline__ float x16sum(float x) {
  auto s = __builtin_amdgcn_permlane16_swap(__float_as_uint(x), __float_as_uint(x), false, false);
  return __uint_as_float(s[0]) + __uint_as_float(s[1]);
}
__device__ __forceinline__ float x32sum(float x) {
  auto t = __builtin_amdgcn_permlane32_swap(__float_as_uint(x), __float_as_uint(x), false, false);
  return __uint_as_float(t[0]) + __uint_as_float(t[1]);
}
__device__ __forceinline__ float x16max(float x) {
  auto s = __builtin_amdgcn_permlane16_swap(__float_as_uint(x), __float_as_uint(x), false, false);
  return fmaxf(__uint_as_float(s[0]), __uint_as_float(s[1]));
}
__device__ __forceinline__ float x32max(float x) {
  auto t = __builtin_amdgcn_permlane32_swap(__float_as_uint(x), __float_as_uint(x), false, false);
  return fmaxf(__uint_as_float(t[0]), __uint_as_float(t[1]));
}
__device__ __forceinline__ float sum_kk(float x) { x += dpp<XOR8>(x); x = x16sum(x); return x32sum(x); }
__device__ __forceinline__ float wave_sum(float x) {
  x += dpp<XOR1>(x); x += dpp<XOR2>(x); x += dpp<XOR7>(x); return sum_kk(x);
}
__device__ __forceinline__ float tr32(float a, float b) {
  auto t = __builtin_amdgcn_permlane32_swap(__float_as_uint(a), __float_as_uint(b), false, false);
  return __uint_as_float(t[0]) + __uint_as_float(t[1]);
}
__device__ __forceinline__ float tr16(float a, float b) {
  auto t = __builtin_amdgcn_permlane16_swap(__float_as_uint(a), __float_as_uint(b), false, false);
  return __uint_as_float(t[0]) + __uint_as_float(t[1]);
}
__device__ __forceinline__ float tr8(float a, float b, bool hi) { return (hi ? b : a) + dpp<XOR8>(hi ? a : b); }
__device__ __forceinline__ unsigned cvtpk(float lo, float hi) {
  return __builtin_bit_cast(unsigned, __builtin_convertvector(f32x2{lo, hi}, bf16x2));
}
__device__ __forceinline__ float rbf(float v) { return bflo(cvtpk(v, 0.f)); }
__device__ __forceinline__ f32x4 mfma(u32x4 a, u32x4 b, f32x4 c) {
  return __builtin_amdgcn_mfma_f32_16x16x32_bf16(__builtin_bit_cast(bf16x8, a), __builtin_bit_cast(bf16x8, b), c, 0, 0, 0);
}
__device__ __forceinline__ u32x4 bfrag(const u32x4* r, int ec) {
  u32x4 f;
#pragma unroll
  for (int p = 0; p < 4; ++p) f[p] = __builtin_amdgcn_perm(r[2 * p + 1][ec >> 1], r[2 * p][ec >> 1], (ec & 1) ? 0x07060302u : 0x05040100u);
  return f;
}
typedef __amdgpu_buffer_rsrc_t rsrc_t;
__device__ __forceinline__ rsrc_t mkrs(const void* p, unsigned n) { return __builtin_amdgcn_make_buffer_rsrc(const_cast<void*>(p), (short)0, (int)n, 0x00020000); }
constexpr int AUX_NT = 2, AUX_SC1 = 16;
__device__ __forceinline__ u32x4 ldb(rsrc_t rs, int voff, int soff) { return __builtin_amdgcn_raw_buffer_load_b128(rs, voff, soff, 0); }
__device__ __forceinline__ u32x4 ldb_nt(rsrc_t rs, int voff, int soff) { return __builtin_amdgcn_raw_buffer_load_b128(rs, voff, soff, AUX_NT); }
__device__ __forceinline__ u32x4 ld_ag(rsrc_t rs, int voff) { return __builtin_amdgcn_raw_buffer_load_b128(rs, voff, 0, AUX_SC1); }
__device__ __forceinline__ void st_ag(rsrc_t rs, int voff, u32x4 v) { __builtin_amdgcn_raw_buffer_store_b128(v, rs, voff, 0, AUX_SC1); }
struct Tag { unsigned t0, t1; };
__device__ __forceinline__ unsigned r22(float v) { return (__float_as_uint(v) + 0x200u) >> 10; }
__device__ __forceinline__ float rnd22(float v) { return __uint_as_float(r22(v) << 10); }
__device__ __forceinline__ u32x4 g_pack(float a, float b, float c, float d, Tag t) {
  const unsigned b2 = r22(b), d2 = r22(d);
  return u32x4{r22(a) | (b2 << 22), (b2 >> 10) | t.t0, r22(c) | (d2 << 22), (d2 >> 10) | t.t1};
}
__device__ __forceinline__ bool g_ok(u32x4 g, Tag t) { return (((g[1] ^ t.t0) | (g[3] ^ t.t1)) >> 12) == 0u; }
__device__ __forceinline__ f32x4 g_val(u32x4 g) {
  return f32x4{__uint_as_float(g[0] << 10), __uint_as_float(__builtin_amdgcn_alignbit(g[1], g[0], 22) << 10),
               __uint_as_float(g[2] << 10), __uint_as_float(__builtin_amdgcn_alignbit(g[3], g[2], 22) << 10)};
}
__device__ __forceinline__ u32x2 h_pack(float a, float b, unsigned t) {
  const int e = max(__float_as_uint(a) & 0x7fffffffu, __float_as_uint(b) & 0x7fffffffu) >> 23;
  const unsigned ma = min((int)__builtin_rintf(__builtin_ldexpf(a, 143 - e)), 131071), mb = min((int)__builtin_rintf(__builtin_ldexpf(b, 143 - e)), 131071);
  return u32x2{(ma & 0x3ffffu) | (mb << 18), ((mb >> 14) & 15u) | (e << 4) | t};
}
__device__ __forceinline__ u32x4 p_pack(f32x4 v, Tag t) {
  const u32x2 lo = h_pack(v[0], v[1], t.t0), hi = h_pack(v[2], v[3], t.t1);
  return u32x4{lo[0], lo[1], hi[0], hi[1]};
}
__device__ __forceinline__ f32x4 p_val(u32x4 g) {
  f32x4 v;
#pragma unroll
  for (int i = 0; i < 4; i += 2) {
    const int e = (int)((g[i + 1] >> 4) & 255u) - 143;
    v[i] = __builtin_ldexpf((float)((int)(g[i] << 14) >> 14), e);
    v[i + 1] = __builtin_ldexpf((float)((int)(__builtin_amdgcn_alignbit(g[i + 1], g[i], 18) << 14) >> 14), e);
  }
  return v;
}
__device__ __forceinline__ unsigned s_exp(float a, float b) { return max(max(__float_as_uint(a) & 0x7f800000u, __float_as_uint(b) & 0x7f800000u), 8u << 23); }
__device__ __forceinline__ unsigned s_two(float a, float b, unsigned ex) {
  const float sc = __uint_as_float(((261u << 23) | 0x7f8000u) - ex);
  return (__float_as_uint(fmaf(a, sc, 12582912.f)) & 0x3ffu) | ((__float_as_uint(fmaf(b, sc, 12582912.f)) & 0x3ffu) << 10);
}
__device__ __forceinline__ void s_acc(float (&o)[8], u32x4 g, float sc, bool first) {
  sc *= __uint_as_float((((g[1] >> 8) & 15u) | ((g[3] >> 4) & 0xf0u)) << 23);
#pragma unroll
  for (int k = 0; k < 2; ++k)
#pragma unroll
    for (int i = 0; i < 4; ++i) {
      const float m = (float)(int)__builtin_amdgcn_sbfe(i < 3 ? g[2 * k] : __builtin_amdgcn_alignbit(g[2 * k + 1], g[2 * k], 30), i < 3 ? 10 * i : 0, 10);
      o[4 * k + i] = first ? m * sc : fmaf(m, sc, o[4 * k + i]);
    }
}
constexpr int SPIN_MAX = 1 << 18;
#define SB() __builtin_amdgcn_sched_barrier(0)
#define FENCE() asm volatile("" ::: "memory")
#define BAR() asm volatile("s_waitcnt lgkmcnt(0)\n\ts_barrier" ::: "memory")
#define VMW(n) asm volatile("s_waitcnt vmcnt(%0)" :: "n"(n) : "memory")
template <int SL> __device__ __forceinline__ void poll5(u32x4 (&r)[5], u32x4 (&p)[15], const int (&f)[5], rsrc_t rs, Tag tag) {
  unsigned t, cnt;
  asm volatile(
      "s_mov_b32 %[cnt], %[mx]\n\t"
      "buffer_load_dwordx4 v[48:51], %[f0], %[rs], 0 offen sc1\n\tbuffer_load_dwordx4 v[52:55], %[f1], %[rs], 0 offen sc1\n\tbuffer_load_dwordx4 v[56:59], %[f2], %[rs], 0 offen sc1\n\tbuffer_load_dwordx4 v[60:63], %[f3], %[rs], 0 offen sc1\n\tbuffer_load_dwordx4 v[64:67], %[f4], %[rs], 0 offen sc1\n\ts_sleep %[sl]\n\t"
      "buffer_load_dwordx4 v[68:71], %[f0], %[rs], 0 offen sc1\n\tbuffer_load_dwordx4 v[72:75], %[f1], %[rs], 0 offen sc1\n\tbuffer_load_dwordx4 v[76:79], %[f2], %[rs], 0 offen sc1\n\tbuffer_load_dwordx4 v[80:83], %[f3], %[rs], 0 offen sc1\n\tbuffer_load_dwordx4 v[84:87], %[f4], %[rs], 0 offen sc1\n\ts_sleep %[sl]\n\t"
      "1%=:\n\t"
      "buffer_load_dwordx4 v[88:91], %[f0], %[rs], 0 offen sc1\n\tbuffer_load_dwordx4 v[92:95], %[f1], %[rs], 0 offen sc1\n\tbuffer_load_dwordx4 v[96:99], %[f2], %[rs], 0 offen sc1\n\tbuffer_load_dwordx4 v[100:103], %[f3], %[rs], 0 offen sc1\n\tbuffer_load_dwordx4 v[104:107], %[f4], %[rs], 0 offen sc1\n\t"
      "s_waitcnt vmcnt(10)\n\tv_xor_b32 %[t], %[t0], v49\n\tv_bitop3_b32 %[t], %[t], %[t1], v51 bitop3:0xf6\n\tv_bitop3_b32 %[t], %[t], %[t0], v53 bitop3:0xf6\n\tv_bitop3_b32 %[t], %[t], %[t1], v55 bitop3:0xf6\n\tv_bitop3_b32 %[t], %[t], %[t0], v57 bitop3:0xf6\n\tv_bitop3_b32 %[t], %[t], %[t1], v59 bitop3:0xf6\n\tv_bitop3_b32 %[t], %[t], %[t0], v61 bitop3:0xf6\n\tv_bitop3_b32 %[t], %[t], %[t1], v63 bitop3:0xf6\n\tv_bitop3_b32 %[t], %[t], %[t0], v65 bitop3:0xf6\n\tv_bitop3_b32 %[t], %[t], %[t1], v67 bitop3:0xf6\n\tv_cmp_le_u32 vcc, %[lim], %[t]\n\tv_mov_b64 v[108:109], v[48:49]\n\tv_mov_b64 v[110:111], v[50:51]\n\tv_mov_b64 v[112:113], v[52:53]\n\tv_mov_b64 v[114:115], v[54:55]\n\tv_mov_b64 v[116:117], v[56:57]\n\tv_mov_b64 v[118:119], v[58:59]\n\tv_mov_b64 v[120:121], v[60:61]\n\tv_mov_b64 v[122:123], v[62:63]\n\tv_mov_b64 v[124:125], v[64:65]\n\tv_mov_b64 v[126:127], v[66:67]\n\ts_mov_b64 exec, vcc\n\ts_cbranch_execz 2%=f\n\t"
      "buffer_load_dwordx4 v[48:51], %[f0], %[rs], 0 offen sc1\n\tbuffer_load_dwordx4 v[52:55], %[f1], %[rs], 0 offen sc1\n\tbuffer_load_dwordx4 v[56:59], %[f2], %[rs], 0 offen sc1\n\tbuffer_load_dwordx4 v[60:63], %[f3], %[rs], 0 offen sc1\n\tbuffer_load_dwordx4 v[64:67], %[f4], %[rs], 0 offen sc1\n\t"
      "s_waitcnt vmcnt(10)\n\tv_xor_b32 %[t], %[t0], v69\n\tv_bitop3_b32 %[t], %[t], %[t1], v71 bitop3:0xf6\n\tv_bitop3_b32 %[t], %[t], %[t0], v73 bitop3:0xf6\n\tv_bitop3_b32 %[t], %[t], %[t1], v75 bitop3:0xf6\n\tv_bitop3_b32 %[t], %[t], %[t0], v77 bitop3:0xf6\n\tv_bitop3_b32 %[t], %[t], %[t1], v79 bitop3:0xf6\n\tv_bitop3_b32 %[t], %[t], %[t0], v81 bitop3:0xf6\n\tv_bitop3_b32 %[t], %[t], %[t1], v83 bitop3:0xf6\n\tv_bitop3_b32 %[t], %[t], %[t0], v85 bitop3:0xf6\n\tv_bitop3_b32 %[t], %[t], %[t1], v87 bitop3:0xf6\n\tv_cmp_le_u32 vcc, %[lim], %[t]\n\tv_mov_b64 v[108:109], v[68:69]\n\tv_mov_b64 v[110:111], v[70:71]\n\tv_mov_b64 v[112:113], v[72:73]\n\tv_mov_b64 v[114:115], v[74:75]\n\tv_mov_b64 v[116:117], v[76:77]\n\tv_mov_b64 v[118:119], v[78:79]\n\tv_mov_b64 v[120:121], v[80:81]\n\tv_mov_b64 v[122:123], v[82:83]\n\tv_mov_b64 v[124:125], v[84:85]\n\tv_mov_b64 v[126:127], v[86:87]\n\ts_mov_b64 exec, vcc\n\ts_cbranch_execz 2%=f\n\t"
      "buffer_load_dwordx4 v[68:71], %[f0], %[rs], 0 offen sc1\n\tbuffer_load_dwordx4 v[72:75], %[f1], %[rs], 0 offen sc1\n\tbuffer_load_dwordx4 v[76:79], %[f2], %[rs], 0 offen sc1\n\tbuffer_load_dwordx4 v[80:83], %[f3], %[rs], 0 offen sc1\n\tbuffer_load_dwordx4 v[84:87], %[f4], %[rs], 0 offen sc1\n\t"
      "s_waitcnt vmcnt(10)\n\tv_xor_b32 %[t], %[t0], v89\n\tv_bitop3_b32 %[t], %[t], %[t1], v91 bitop3:0xf6\n\tv_bitop3_b32 %[t], %[t], %[t0], v93 bitop3:0xf6\n\tv_bitop3_b32 %[t], %[t], %[t1], v95 bitop3:0xf6\n\tv_bitop3_b32 %[t], %[t], %[t0], v97 bitop3:0xf6\n\tv_bitop3_b32 %[t], %[t], %[t1], v99 bitop3:0xf6\n\tv_bitop3_b32 %[t], %[t], %[t0], v101 bitop3:0xf6\n\tv_bitop3_b32 %[t], %[t], %[t1], v103 bitop3:0xf6\n\tv_bitop3_b32 %[t], %[t], %[t0], v105 bitop3:0xf6\n\tv_bitop3_b32 %[t], %[t], %[t1], v107 bitop3:0xf6\n\tv_cmp_le_u32 vcc, %[lim], %[t]\n\tv_mov_b64 v[108:109], v[88:89]\n\tv_mov_b64 v[110:111], v[90:91]\n\tv_mov_b64 v[112:113], v[92:93]\n\tv_mov_b64 v[114:115], v[94:95]\n\tv_mov_b64 v[116:117], v[96:97]\n\tv_mov_b64 v[118:119], v[98:99]\n\tv_mov_b64 v[120:121], v[100:101]\n\tv_mov_b64 v[122:123], v[102:103]\n\tv_mov_b64 v[124:125], v[104:105]\n\tv_mov_b64 v[126:127], v[106:107]\n\ts_mov_b64 exec, vcc\n\ts_cbranch_execz 2%=f\n\t"
      "s_sub_u32 %[cnt], %[cnt], 1\n\ts_cmp_lg_u32 %[cnt], 0\n\ts_cbranch_scc1 1%=b\n2%=:\n\ts_mov_b64 exec, -1"
      : "=&{v[108:111]}"(r[0]), "=&{v[112:115]}"(r[1]), "=&{v[116:119]}"(r[2]), "=&{v[120:123]}"(r[3]), "=&{v[124:127]}"(r[4]),
        "=&{v[48:51]}"(p[0]), "=&{v[52:55]}"(p[1]), "=&{v[56:59]}"(p[2]), "=&{v[60:63]}"(p[3]), "=&{v[64:67]}"(p[4]), "=&{v[68:71]}"(p[5]), "=&{v[72:75]}"(p[6]), "=&{v[76:79]}"(p[7]), "=&{v[80:83]}"(p[8]), "=&{v[84:87]}"(p[9]), "=&{v[88:91]}"(p[10]), "=&{v[92:95]}"(p[11]), "=&{v[96:99]}"(p[12]), "=&{v[100:103]}"(p[13]), "=&{v[104:107]}"(p[14]),
        [t] "=&v"(t), [cnt] "=&s"(cnt)
      : [f0] "v"(f[0]), [f1] "v"(f[1]), [f2] "v"(f[2]), [f3] "v"(f[3]), [f4] "v"(f[4]), [rs] "s"(rs), [t0] "s"(tag.t0), [t1] "s"(tag.t1), [lim] "s"(4096u), [mx] "n"(SPIN_MAX), [sl] "n"(SL)
      : "vcc", "scc", "memory");
}
__device__ __forceinline__ void poll5_keep(u32x4 (&p)[15]) { asm volatile("" :: "{v[48:51]}"(p[0]), "{v[52:55]}"(p[1]), "{v[56:59]}"(p[2]), "{v[60:63]}"(p[3]), "{v[64:67]}"(p[4]), "{v[68:71]}"(p[5]), "{v[72:75]}"(p[6]), "{v[76:79]}"(p[7]), "{v[80:83]}"(p[8]), "{v[84:87]}"(p[9]), "{v[88:91]}"(p[10]), "{v[92:95]}"(p[11]), "{v[96:99]}"(p[12]), "{v[100:103]}"(p[13]), "{v[104:107]}"(p[14])); }
template <int SL> __device__ __forceinline__ void look2(u32x4 (&r)[2], const int (&f)[2], rsrc_t rs, Tag tag) {
  unsigned t, cnt = SPIN_MAX, lim = 4095u;
  asm volatile("s_branch 2%=f\n1%=:\n\ts_sleep %[sl]\n2%=:\n\t"
               "buffer_load_dwordx4 %[r0], %[f0], %[rs], 0 offen sc1\n\tbuffer_load_dwordx4 %[r1], %[f1], %[rs], 0 offen sc1\n\t"
               "s_sub_u32 %[cnt], %[cnt], 1\n\ts_cselect_b32 %[lim], -1, %[lim]\n\ts_waitcnt vmcnt(0)\n\t"
               "v_xor_b32 %[t], %[t0], v121\n\tv_bitop3_b32 %[t], %[t], %[t1], v123 bitop3:0xf6\n\tv_bitop3_b32 %[t], %[t], %[t0], v125 bitop3:0xf6\n\tv_bitop3_b32 %[t], %[t], %[t1], v127 bitop3:0xf6\n\t"
               "v_cmp_lt_u32 vcc, %[lim], %[t]\n\ts_mov_b64 exec, vcc\n\ts_cbranch_vccnz 1%=b\n\ts_mov_b64 exec, -1"
               : [r0] "=&{v[120:123]}"(r[0]), [r1] "=&{v[124:127]}"(r[1]), [t] "=&v"(t), [cnt] "+s"(cnt), [lim] "+s"(lim)
               : [f0] "v"(f[0]), [f1] "v"(f[1]), [rs] "s"(rs), [t0] "s"(tag.t0), [t1] "s"(tag.t1), [sl] "n"(SL) : "vcc", "scc", "memory");
}
#define LDSWAITV(fs_, word, n, slp) do { unsigned fv_; \
    asm volatile("1%=:\n\tds_read_b32 %0, %2\n\ts_waitcnt lgkmcnt(0)\n\tv_readfirstlane_b32 %1, %0\n\ts_cmp_lt_u32 %1, %3\n\ts_cbranch_scc0 2%=f\n\ts_sleep %4\n\ts_branch 1%=b\n2%=:" \
                 : "=&v"(fv_), "=&s"(fs_) : "v"((unsigned)(uintptr_t)&(word)), "n"(n), "n"(slp) : "scc", "memory"); } while (0)
#define LDSADD(word, n) asm volatile("s_mov_b64 exec, 1\n\tds_add_u32 %0, %1\n\ts_mov_b64 exec, -1" :: "v"((unsigned)(uintptr_t)&(word)), "v"(n) : "memory")
#define LDSINC(word) LDSADD(word, 1u)
#define LDSWAIT(word, n, slp) do { unsigned fs0_; LDSWAITV(fs0_, word, n, slp); } while (0)

constexpr int ASTR = 104;

__global__ void __launch_bounds__(768, 3)
fused_k(const bf16*, const bf16*, const bf16*, const bf16*, const bf16*, const bf16*, const bf16*, const bf16*, unsigned char*, bf16*) {
  u32x16 ka; u32x8 kb;
  asm volatile("s_load_dwordx16 s[64:79], %2, 0x0\n\ts_load_dwordx4 s[80:83], %2, 0x40\n\ts_memrealtime s[84:85]"
               : "=&{s[64:79]}"(ka), "=&{s[80:87]}"(kb) : "s"(__builtin_amdgcn_kernarg_segment_ptr()));
  SB();
  asm volatile("s_load_dwordx2 s[86:87], %1, %2" : "+{s[80:87]}"(kb)
               : "s"(__builtin_amdgcn_kernarg_segment_ptr()), "s"((unsigned)(0x1197444480653333ull >> ((blockIdx.x >> 4) * 4)) % 16 * 8));
#define KARG(lo, hi) ({ u64 p_; asm volatile("s_mov_b64 %0, s[" #lo ":" #hi "]" : "=s"(p_) : "{s[64:79]}"(ka), "{s[80:87]}"(kb)); p_; })
#define GPTR(T, lo, hi) ((T*)(__attribute__((address_space(1))) T*)KARG(lo, hi))
#define ARGS() asm volatile("s_waitcnt lgkmcnt(0)")
#define ARGS2() ws = GPTR(unsigned char, 80, 81); rw = mkrs(ws, WS_END)

  __shared__ __attribute__((aligned(16))) float stat[NB][2];
  __shared__ __attribute__((aligned(16))) unsigned qs[NB][DH / 2];
  __shared__ __attribute__((aligned(16))) unsigned ctl[16];
  __shared__ __attribute__((aligned(16))) unsigned ofs[32], lat[8];
  __shared__ __attribute__((aligned(16))) unsigned pl[8][8];
  __shared__ __attribute__((aligned(16))) unsigned tw0, tb4[4];
  __shared__ __attribute__((aligned(16))) float mrg[NB][16][DH];
  __shared__ __attribute__((aligned(16))) float mlw[4][32];
  __shared__ __attribute__((aligned(16))) unsigned oA[4 * NB][ASTR];
  __shared__ __attribute__((aligned(256))) u32x4 wf[DH][16];
  float (*red)[NB][64] = reinterpret_cast<float (*)[NB][64]>(&wf[0][0]);
  u64 pc0;
  asm volatile("s_getpc_b64 %0" : "=s"(pc0));
  const int h = blockIdx.x & 31, sp = blockIdx.x >> 5;
  const int tid = threadIdx.x, lane = tid & 63, wave = __builtin_amdgcn_readfirstlane(tid >> 6);
  unsigned char* ws;
  rsrc_t rw;
  Tag tag;
  u64 epoch = 0;
  unsigned role;
  auto mkrole = [&]() { unsigned u = blockIdx.x; asm volatile("" : "+s"(u)); u = 8 * (u & 31) + (u >> 5); role = u + 1 + (u < 160 ? 155 - 10 * (u / 5) : 0); };
  u64 tb;
  u32x4 wv[12];
  if (wave < 4) {
    if (PRS) __builtin_amdgcn_s_setprio(PRS);
    const int fu = 3 * h + sp - 5, cb = fu >> 1, c3 = cb >= 24;
    const int qm = -(int)(sp < 5), s0 = 5 * (fu & 1) + (qm & (6 * sp - 5 * (fu & 1)));
    int ld2 = FF * 2 + (qm & (NH * DH * 2 - FF * 2));
    int vo = ((lane & 8) * 4 + (lane >> 4) * 8) * ld2 + 16 * (lane & 7);
    const int qa = ((s0 + wave) * 26) >> 8, ra = s0 + wave - 10 * qa, am = sp < 5 ? 6 : 10 + 2 * c3;
    int soa = 64 * ra * ld2 + 128 * qa + 128 * (cb - 24 * c3) + (qm & (h * DH * 2 - 128 * (cb - 24 * c3)));
    asm volatile("" : "+v"(vo));
    asm volatile("" : "+s"(ld2)); asm volatile("" : "+s"(soa));
    u64 up;
    asm volatile("s_mov_b32 m0, %1\n\ts_waitcnt lgkmcnt(0)\n\ts_movrels_b64 %0, s[64:65]" : "=s"(up) : "s"(am), "{s[64:79]}"(ka));
    const rsrc_t ru = mkrs((const void*)(__attribute__((address_space(1))) const void*)up, DM * ld2);
#pragma unroll
    for (int e = 0; e < 8; ++e) {
      wv[e] = ldb_nt(ru, vo, soa + e * ld2); SB();
      if (e == BPOS - 1) asm volatile("s_memrealtime %0" : "=s"(tb));
    }
    {
      const int gb = s0 + 4 + (wave >> 1), qb = (gb * 26) >> 8, rb = gb - 10 * qb;
      const rsrc_t r2 = mkrs((const void*)(__attribute__((address_space(1))) const void*)up, sp < 5 || wave < 2 ? DM * ld2 : 0);
      const int sob = soa + (64 * (rb - ra) + 4 * (wave & 1)) * ld2 + 128 * (qb - qa);
#pragma unroll
      for (int e = 0; e < 4; ++e) { wv[8 + e] = ldb_nt(r2, vo, sob + e * ld2); SB(); }
    }
  }
  auto origin = [&]() {
    const unsigned te = tw0;
    const u32x4 tq = *reinterpret_cast<const u32x4*>(tb4);
    const int dl = max(max((int)(tq[0] - te), (int)(tq[1] - te)), max((int)(tq[2] - te), (int)(tq[3] - te)));
    return __builtin_amdgcn_readfirstlane(te + ((max(dl - OB, 0) * OK8) >> 3));
  };
  int wsel = wave;
  asm volatile("" : "+s"(wsel));
  if (__builtin_expect(wsel >= 4, 1)) {
    __builtin_amdgcn_s_waitcnt(0x0f70);
    const int w = wave - 4, kk = lane >> 3, sub = lane & 7;
    unsigned wbase = (h * SK + (SK - WIN)) * DH;
    int voff = ((sp * 8 + w) * 8 + kk) * (DH * 2) + 16 * sub;
    asm volatile("" : "+v"(voff));
    asm volatile("" : "+s"(wbase));
    u64 tz;
    asm volatile("" : "={s[88:89]}"(tz));
    if (w == 1 && sp < 7) {
      const int ix = blockIdx.x >> 3;
      const unsigned to = (ix & 8) == 0 ? (ix & 7) * 983040 : (0x2008a00u >> ix) & 1 ? DM * FF * 2 - 64 : 128;
      asm volatile("s_waitcnt lgkmcnt(0)\n\ts_load_dword s88, s[86:87], %0" :: "s"(to), "{s[80:87]}"(kb), "{s[88:89]}"(tz) : "memory");
    } else ARGS();
    const rsrc_t rk = mkrs(GPTR(const bf16, 66, 67) + wbase, 3 * BSTR + WIN * DH * 2);
    ARGS2();
    const rsrc_t rv = mkrs(GPTR(const bf16, 68, 69) + wbase, 3 * BSTR + WIN * DH * 2);
    u32x4 tl[NBUF][3];
    unsigned tstart;
    unsigned late = 0;
    const unsigned ofa = (unsigned)(uintptr_t)&ofs[0], laa = (unsigned)(uintptr_t)&lat[w];
#define PACE(t) do { { \
      if (NCK && (t) >= CK0 - 1) { unsigned tv_; \
        asm volatile("1%=:\n\tds_read_b32 %1, %4 offset:%5\n\ts_memrealtime s[90:91]\n\ts_waitcnt lgkmcnt(0)\n\tv_readfirstlane_b32 s91, %1\n\ts_sub_u32 %0, s90, %2\n\t" \
                     "s_addk_i32 %0, %3\n\ts_sub_u32 %0, %0, s91\n\ts_cmp_lt_i32 %0, 0\n\ts_cbranch_scc0 2%=f\n\ts_sleep 1\n\ts_branch 1%=b\n2%=:\n\t" \
                     "v_mov_b32 %1, %0\n\ts_mov_b64 exec, 1\n\tds_write_b32 %6, %1\n\ts_mov_b64 exec, -1" \
                     : "=&s"(late), "=&v"(tv_) : "s"(tstart), "n"(-sched(t)), "v"(ofa), "n"(4 * (t)), "v"(laa) : "scc", "s90", "s91", "memory"); \
      } else \
      asm volatile("1%=:\n\ts_memrealtime s[90:91]\n\ts_waitcnt lgkmcnt(0)\n\ts_sub_u32 %0, s90, %1\n\ts_addk_i32 %0, %2\n\ts_cmp_lt_i32 %0, 0\n\ts_cbranch_scc0 2%=f\n\ts_sleep 1\n\ts_branch 1%=b\n2%=:" \
                   : "=&s"(late) : "s"(tstart), "n"(-sched(t)) : "scc", "s90", "s91", "memory"); \
      if (PR1) asm volatile("s_setprio 0\n\ts_cmpk_lt_u32 %0, %1\n\ts_cbranch_scc1 1%=f\n\ts_setprio 1\n\ts_cmpk_lt_u32 %0, %2\n\ts_cbranch_scc1 1%=f\n\t" \
                   "s_setprio 2\n\ts_cmpk_lt_u32 %0, %3\n\ts_cbranch_scc1 1%=f\n\ts_setprio 3\n1%=:" \
                   :: "s"(late), "n"(PR1), "n"(PR2), "n"(PR3) : "scc", "memory"); } } while (0)
#define WINDOW(t) do { if (LATE && (t) < TSW) \
      asm volatile("s_cmpk_lt_u32 %0, %1\n\ts_cbranch_scc0 1%=f\n\ts_waitcnt vmcnt(%2)\n1%=:\n\ts_waitcnt vmcnt(%3)" \
                   :: "s"(late), "n"(LATE), "n"(WINL - 1), "n"(WINB - 1) : "scc", "memory"); \
      else VMW(((t) < TSW ? WINL : WINV) - 1); } while (0)
#define ISSUE_T(t) do { PACE(t); _Pragma("unroll") for (int i_ = 0; i_ < 3; ++i_) { \
      WINDOW(t); \
      tl[(t) % NBUF][i_] = ldb_nt((t) < NT ? rk : rv, voff + 128 * i_, bat(t) * BSTR + ((t) & 3) * TSTR); \
      SB(); } } while (0)
    SB();
    if (w == 0) { if (lane < 14) ctl[2 + lane] = 0u; if (lane < 32) ofs[lane] = 0u; else lat[lane & 7] = 0u; asm volatile("s_waitcnt lgkmcnt(0)" ::: "memory"); }
    asm volatile("s_buffer_load_dword s89, %0, %1" :: "s"(sp < 4 ? rk : rv), "s"((sp & 3) * BSTR + w * 112344), "{s[88:89]}"(tz) : "memory");
    asm volatile("s_barrier" ::: "memory");
    {
      tstart = origin() + TOFS + ((h * SPAN) >> 5);
      asm volatile("s_waitcnt lgkmcnt(0)" :: "{s[88:89]}"(tz), "s"(tstart) : "memory");
    }
#pragma unroll
    for (int t = 0; t < NBUF; ++t) ISSUE_T(t);
    SB();
    LDSWAIT(ctl[13], 1, 1);
    tstart = __builtin_amdgcn_readfirstlane(ctl[15]) + TOFS + ((h * SPAN) >> 5);
    u32x4 qp[3];
    float sc[NT], ls[NB], ms = 0.f;
#pragma unroll
    for (int t = 0; t < NT; ++t) {
      SB();
      if ((t & 3) == 0) {
#pragma unroll
        for (int i = 0; i < 3; ++i) qp[i] = *reinterpret_cast<const u32x4*>(&qs[bat(t)][32 * i + 4 * sub]);
      }
      float d0 = 0.f, d1 = 0.f;
#pragma unroll
      for (int i = 0; i < 3; ++i) {
        d0 = dot2(tl[t % NBUF][i][0], qp[i][0], d0); d1 = dot2(tl[t % NBUF][i][1], qp[i][1], d1);
        d0 = dot2(tl[t % NBUF][i][2], qp[i][2], d0); d1 = dot2(tl[t % NBUF][i][3], qp[i][3], d1);
      }
      asm volatile("" : "+v"(d0), "+v"(d1));
      SB();
      ISSUE_T(t + NBUF);
      SB();
      float d = d0 + d1;
      d += dpp<XOR1>(d); d += dpp<XOR2>(d); d += dpp<XOR7>(d);
      sc[t] = rbf(rbf(d) * SCALE);
      if ((t & 3) == 3) {
        float mm = fmaxf(fmaxf(sc[t - 3], sc[t - 2]), fmaxf(sc[t - 1], sc[t]));
        mm = fmaxf(mm, dpp<XOR8>(mm)); mm = x16max(mm); mm = x32max(mm);
        mm *= LOG2E;
#pragma unroll
        for (int u = t - 3; u <= t; ++u) sc[u] = __builtin_amdgcn_exp2f(fmaf(sc[u], LOG2E, -mm));
        ls[bat(t)] = (sc[t - 3] + sc[t - 2]) + (sc[t - 1] + sc[t]);
        ms = kk == bat(t) ? mm : ms;
      }
    }
    {
      const float l = x32sum(tr16(tr8(ls[0], ls[1], lane & 8), tr8(ls[2], ls[3], lane & 8)));
      asm volatile("s_mov_b64 exec, 0x01010101\n\tds_write2_b32 %0, %1, %2 offset1:32\n\ts_mov_b64 exec, -1"
                   :: "v"((unsigned)(uintptr_t)&mlw[0][(lane & 24) + w]), "v"(ms), "v"(l) : "memory");
    }
    LDSINC(ctl[4]);
    mkrole();
    u32x4 msk;
#pragma unroll
    for (int r = 0; r < 4; ++r) msk[r] = (sub >> 1) == r ? ((sub & 1) ? 0xffff0000u : 0x0000ffffu) : 0u;
    f32x4 acc[3];
    unsigned mwr = (unsigned)(uintptr_t)&mrg[0][2 * w + (lane >> 5)][32 * ((lane >> 4) & 1) + 4 * sub];
    unsigned mrd = (unsigned)(uintptr_t)&mrg[0][0][4 * (lane < 48 ? lane : 0)];
    int opub = WS_O + (h * NB * 8 + sp) * (DH * 2) + 16 * ((lane >> 4) * 8 + sub);
#pragma unroll
    for (int i = 0; i < 3; ++i) acc[i] = f32x4{0.f, 0.f, 0.f, 0.f};
    tag.t0 = __builtin_amdgcn_readfirstlane(ctl[0]); tag.t1 = __builtin_amdgcn_readfirstlane(ctl[1]);
    asm volatile("" : "+v"(mwr), "+v"(mrd), "+v"(opub));
    constexpr int BL = bat(31), TL = vdone(31);
    auto vstep = [&](int t) {
      const unsigned pd = cvtpk(sc[kpos(bat(t + NT)) + (t & 3)], sc[kpos(bat(t + NT)) + (t & 3)]);
      const u32x4 af = {pd & msk[0], pd & msk[1], pd & msk[2], pd & msk[3]};
#pragma unroll
      for (int i = 0; i < 3; ++i) acc[i] = mfma(af, tl[(t + NT) % NBUF][i], acc[i]);
    };
#define DEPOSIT(b) asm volatile("s_mov_b64 exec, %[vm]\n\ts_nop 7\n\tds_write_b128 %[a], %[x0] offset:%[o0]\n\tds_write_b128 %[a], %[x1] offset:%[o1]\n\tds_write_b128 %[a], %[x2] offset:%[o2]\n\t" \
                     "s_mov_b64 exec, -1" :: [vm] "s"(0xff00ff0000ff00ffull), [a] "v"(mwr), [x0] "v"(acc[0]), [x1] "v"(acc[1]), [x2] "v"(acc[2]), \
                        [o0] "n"((b) * 12288), [o1] "n"((b) * 12288 + 256), [o2] "n"((b) * 12288 + 512) : "memory")
#pragma unroll
    for (int t = 0; t < TL; ++t) {
      SB();
      if (t == 8) {
        LDSWAIT(ctl[12], 1, 1);
        const float fl = mlw[3][8 * BL + w];
#pragma unroll
        for (int u = 0; u < 4; ++u) sc[kpos(BL) + u] *= fl;
        pl[kk][w] = cvtpk(sc[kpos(BL) + 3], sc[kpos(BL) + 3]);
        SB();
      }
      vstep(t);
      SB();
      if ((t & 3) == 3) {
        DEPOSIT(bat(t + NT));
        if (t == 11) LDSINC(ctl[10]);
#pragma unroll
        for (int i = 0; i < 3; ++i) acc[i] = f32x4{0.f, 0.f, 0.f, 0.f};
      }
#pragma unroll
      for (int u = NT + NBUF; u < 2 * NT - 1; ++u)
        if (vdone(u) == t + 1) {
          ISSUE_T(u);
        }
      SB();
    }
    static_assert(TL > 11 && TL <= 15 && vdone(30) <= TL, "");
    typedef __attribute__((address_space(3))) const f32x4* lp4;
    u32x4 og = {0u, 0u, 0u, 0u};
    u64 om = w < 3 ? 0xffull : w < 6 ? 0x00ff00ff00ffull : 0ull;
    unsigned c5 = (unsigned)(uintptr_t)&ctl[5], i5 = w < 3;
    u32x4 rq = {(unsigned)(uintptr_t)ws, (unsigned)((uintptr_t)ws >> 32) & 0xffffu, (unsigned)WS_END, 0x00020000u};
    asm volatile("" : "+s"(om)); asm volatile("" : "+s"(rq)); asm volatile("" : "+v"(c5), "+v"(i5));
    if (w >= 6) {
      const int hp = (role - 1) / 5;
      const rsrc_t ru = mkrs((role <= 160 ? GPTR(const bf16, 72, 73) : GPTR(const bf16, 78, 79) - NH * DH * DM) + (size_t)(hp * DH + 32 * (w - 6)) * DM, role <= 200 ? 160 * DM * 2 : 0);
      int vo = (lane >> 4) * (DM * 2) + 256 * (role - 1 - 5 * hp) + 16 * ((lane & 15) ^ ((lane >> 4) * 4));
      asm volatile("" : "+v"(vo));
      unsigned tw = tstart + sched(31) + (role <= 160 ? WD1 : WDF);
      asm volatile("" : "+s"(tw));
#pragma unroll
      for (int t = TL; t < NT - 1; ++t) { SB(); vstep(t); }
      SB();
      DEPOSIT(BL);
      LDSINC(ctl[14]);
      if (role <= 200) {
      tw += __builtin_amdgcn_readfirstlane(ofs[31]);
      if (LA) tw += max((int)((unsigned)__builtin_amdgcn_s_memrealtime() - tstart - __builtin_amdgcn_readfirstlane(ofs[30])) - sched(30) - LA, 0);
      while ((int)((unsigned)__builtin_amdgcn_s_memrealtime() - tw) < 0) __builtin_amdgcn_s_sleep(1);
#pragma unroll
      for (int e = 0; e < 24; ++e) {
        if (e && e % 8 == 0) { SB(); __builtin_amdgcn_s_sleep(WD2); SB(); }
        const int row = 64 * (e >> 3) + 4 * (e & 7);
        __builtin_amdgcn_raw_ptr_buffer_load_lds(ru, (__attribute__((address_space(3))) void*)&wf[32 * (w - 6) + row][0], 16, vo ^ (16 * (e & 3)), row * (DM * 2), 0, AUX_NT);
      }
      SB();
      VMW(8);
      LDSINC(ctl[6]);
      VMW(0);
      LDSINC(ctl[11]);
      }
    } else if (w < 3) {
      u32x4 lt[8];
      PACE(31);
#pragma unroll
      for (int i = 0; i < 8; ++i) { WINDOW(31); lt[i] = ldb_nt(rv, voff, BL * BSTR + 3 * TSTR + 8 * DH * 2 * i - w * (8 * DH * 2 - 128)); SB(); }
      LDSINC(ctl[8]);
#pragma unroll
      for (int t = TL; t < NT - 1; ++t) { SB(); vstep(t); }
      SB();
      DEPOSIT(BL);
      LDSINC(ctl[14]);
      mrd = mwr + BL * (16 * DH * 4) - w * (2 * DH * 4 - 256);
      opub = WS_O + ((h * NB + BL) * 8 + sp) * (DH * 2) + 16 * (8 * w + sub);
      LDSWAIT(ctl[14], 8, 1);
      const u32x4 pa = *reinterpret_cast<const u32x4*>(&pl[kk][0]), pb = *reinterpret_cast<const u32x4*>(&pl[kk][4]);
      f32x4 pv[8];
#pragma unroll
      for (int i = 0; i < 8; ++i) pv[i] = *reinterpret_cast<lp4>(mrd + i * (2 * DH * 4));
      u32x4 af[8];
#pragma unroll
      for (int i = 0; i < 8; ++i) {
        const unsigned pd = (i < 4 ? pa : pb)[i & 3];
        af[i] = u32x4{pd & msk[0], pd & msk[1], pd & msk[2], pd & msk[3]};
        asm volatile("" : "+v"(af[i]));
      }
      acc[0] = (pv[0] + pv[1]) + (pv[2] + pv[3]); acc[1] = (pv[4] + pv[5]) + (pv[6] + pv[7]);
      unsigned tv = lane & 16 ? tag.t1 : tag.t0, tn = lane & 16 ? 27u : 23u, j0 = 0u, j1 = 0u, j2 = 0u;
      asm volatile("" : "+v"(acc[0]), "+v"(acc[1]), "+v"(opub), "+v"(tv), "+v"(tn), "+v"(j0), "+v"(j1), "+v"(j2));
      SB();
#pragma unroll
      for (int i = 0; i < 8; ++i) acc[i & 1] = mfma(af[i], lt[i], acc[i & 1]);
      acc[0] += acc[1];
      const auto s0 = __builtin_amdgcn_permlane32_swap(__float_as_uint(acc[0][0]), __float_as_uint(acc[0][2]), false, false);
      const auto s1 = __builtin_amdgcn_permlane32_swap(__float_as_uint(acc[0][1]), __float_as_uint(acc[0][3]), false, false);
      const float za = __uint_as_float(s0[0]) + dpp<XOR8>(__uint_as_float(s0[1])), zb = __uint_as_float(s1[0]) + dpp<XOR8>(__uint_as_float(s1[1]));
      unsigned ex = s_exp(za, zb);
      const auto e0 = __builtin_amdgcn_permlane32_swap(ex, ex, false, false);
      ex = max(e0[0], e0[1]);
      const auto e1 = __builtin_amdgcn_permlane16_swap(ex, ex, false, false);
      ex = max(e1[0], e1[1]);
      const auto s2 = __builtin_amdgcn_permlane32_swap(s_two(za, zb, ex), j0, false, false);
      const auto s3 = __builtin_amdgcn_permlane16_swap(s2[0] | (s2[1] << 20), j1, false, false);
      const auto s4 = __builtin_amdgcn_permlane16_swap((s2[1] >> 12) | (__builtin_amdgcn_ubfe(ex, tn, 4) << 8) | tv, j2, false, false);
      og = u32x4{s3[0], s4[0], s3[1], s4[1]};
    } else {
#pragma unroll
      for (int t = TL; t < NT - 1; ++t) { SB(); vstep(t); }
      SB();
      DEPOSIT(BL);
      LDSINC(ctl[14]);
      {
        const unsigned pb = (VORD >> (4 * (w - 3))) & 3u;
        f32x4 pv[16];
        LDSWAIT(ctl[10], 8, 1);
        mrd += pb * (16 * DH * 4); opub += pb * (8 * DH * 2);
        const f32x4 fa = *reinterpret_cast<const f32x4*>(&mlw[3][8 * pb]), fb = *reinterpret_cast<const f32x4*>(&mlw[3][8 * pb + 4]);
#pragma unroll
        for (int i = 0; i < 16; ++i) pv[i] = *reinterpret_cast<lp4>(mrd + i * (DH * 4));
        SB();
#pragma unroll
        for (int i = 0; i < 16; i += 2) pv[i] += pv[i + 1];
#pragma unroll
        for (int i = 0; i < 16; i += 4) pv[i] = (i < 8 ? fa : fb)[(i >> 1) & 3] * pv[i] + (i < 8 ? fa : fb)[((i >> 1) & 3) + 1] * pv[i + 2];
        pv[0] = (pv[0] + pv[4]) + (pv[8] + pv[12]);
        unsigned ex = max(s_exp(pv[0][0], pv[0][1]), s_exp(pv[0][2], pv[0][3]));
        ex = max(ex, __float_as_uint(dpp<XOR8>(__uint_as_float(ex))));
        const unsigned hi = s_two(pv[0][2], pv[0][3], ex);
        og[0] = s_two(pv[0][0], pv[0][1], ex) | (hi << 20);
        og[1] = (hi >> 12) | (lane & 8 ? ((ex >> 27) << 8) | tag.t1 : (((ex >> 23) & 15u) << 8) | tag.t0);
        og[2] = __float_as_uint(dpp<XOR8>(__uint_as_float(og[0]))); og[3] = __float_as_uint(dpp<XOR8>(__uint_as_float(og[1])));
        asm volatile("" : "+v"(og), "+v"(opub));
        if (EPB) LDSWAIT(ctl[5], EPB, 1);
      }
    }
    __builtin_amdgcn_s_waitcnt(0x0f70);
    asm volatile("s_mov_b64 exec, %3\n\tbuffer_store_dwordx4 %0, %1, %2, 0 offen sc1\n\ts_mov_b64 exec, 1\n\tds_add_u32 %4, %5\n\ts_mov_b64 exec, -1"
                 :: "v"(og), "v"(opub), "s"(rq), "s"(om), "v"(c5), "v"(i5) : "memory");
    if (role <= 160 && w < 6) {
      const int hp = (role - 1) / 5;
      const int hf = w >= 3, bb = lane >> 4, pg = 2 * hf + ((lane >> 3) & 1), gq = 8 * (w - 3 * hf) + sub;
      const int f0 = WS_O + ((hp * NB + bb) * 8 + 2 * pg) * (DH * 2) + 16 * gq;
      const int f[2] = {f0, f0 + DH * 2};
      unsigned awr = (unsigned)(uintptr_t)&oA[4 * pg + bb][4 * gq];
      u32x4 r[2];
      LDSWAIT(ctl[9], 1, 1);
      f32x2 sq = *reinterpret_cast<const f32x2*>(&mlw[2][8 * bb + 2 * pg]);
      asm volatile("" : "+v"(sq), "+v"(awr));
      const unsigned tpo = tstart - ((h * SPAN) >> 5) + __builtin_amdgcn_readfirstlane(ofs[31]);
      const int tdo = min(max((int)(__builtin_amdgcn_readfirstlane(ctl[3]) - __builtin_amdgcn_readfirstlane(ctl[15])), -100), 200);
      LDSWAIT(ctl[5], 3, 1);
      if (PT1) while ((unsigned)__builtin_amdgcn_s_memrealtime() - tpo < (unsigned)(sched(31) + max(PT1 + ((hp * SPAN) >> 5) + tdo, PA))) __builtin_amdgcn_s_sleep(1);
      look2<0>(r, f, rw, tag);
      float o[8];
      s_acc(o, r[0], sq[0], true); s_acc(o, r[1], sq[1], false);
      *reinterpret_cast<__attribute__((address_space(3))) u32x4*>(awr) = u32x4{cvtpk(o[0], o[1]), cvtpk(o[2], o[3]), cvtpk(o[4], o[5]), cvtpk(o[6], o[7])};
      LDSADD(ctl[11], 4u);
    }
    if (w == 3 && role - 161u < 80u) {
      const int f = role - 161;
      int oo = ((f & 3) * DM + 32 * (f >> 2) + 4 * (lane & 7)) * 2;
      const unsigned xv = __builtin_amdgcn_raw_buffer_load_b32(mkrs(GPTR(const bf16, 64, 65), NB * DM * 2), oo + 4 * (lane >> 5), 0, 0);
      float x0 = bflo(xv), x1 = bfhi(xv);
      const int yo = WS_Y + ((f >> 4) * (NH + 8) + (lane >> 3)) * 2048 +
                     (f & 15) * 128 + (lane & 7) * 16;
      int fy[5] = {yo, yo + 8 * 2048, yo + 16 * 2048, yo + 24 * 2048, yo + 32 * 2048};
      const u64 po = KARG(82, 83);
      u32x4 ro = {(unsigned)po, (unsigned)(po >> 32) & 0xffffu, (unsigned)(NB * DM * 2), 0x00020000u};
      asm volatile("" : "+v"(x0), "+v"(x1), "+v"(oo), "+v"(fy[0]), "+v"(fy[1]), "+v"(fy[2]), "+v"(fy[3]), "+v"(fy[4])); asm volatile("" : "+s"(ro));
      LDSWAIT(ctl[5], 3, 1);
      const unsigned tpo = tstart - ((h * SPAN) >> 5) + __builtin_amdgcn_readfirstlane(ofs[31]) + sched(31) + PT2;
      unsigned tpl = tpo;
      {
        const u32x4 go = ld_ag(rw, WS_ML + (lane & 31) * 1024 + 32);
        if (g_ok(go, tag)) tpl += min(max((int)(go[0] - ctl[15]), -100), 200);
      }
      while (__all((int)((unsigned)__builtin_amdgcn_s_memrealtime() - tpl) >= 0) == 0) __builtin_amdgcn_s_sleep(1);
      u32x4 p[15], r[5];
      poll5<PSL>(r, p, fy, rw, tag);
      const f32x4 v = (g_val(r[0]) + g_val(r[1])) + (g_val(r[2]) + g_val(r[3])) + g_val(r[4]);
      float a0 = x16sum(tr32(v[0], v[2])), a1 = x16sum(tr32(v[1], v[3]));
      a0 += dpp<XOR8>(a0); a1 += dpp<XOR8>(a1);
      const unsigned r0 = cvtpk(x0 + a0, x1 + a1);
      const auto r1 = __builtin_amdgcn_permlane32_swap(r0, r0, false, false);
      asm volatile("s_mov_b64 exec, 0xff\n\tbuffer_store_dwordx2 %0, %1, %2, 0 offen sc1\n\ts_mov_b64 exec, -1"
                   :: "v"(u32x2{r1[0], r1[1]}), "v"(oo), "s"(ro) : "memory");
      poll5_keep(p);
    }
  } else {
    const int w = wave;
    const int j = lane & 15, g = lane >> 4;
    const bool isq = sp < 5;
    const int fu = 3 * h + sp - 5, cb = fu >> 1, s0 = isq ? 6 * sp : 5 * (fu & 1), qa = ((s0 + w) * 26) >> 8, ra = s0 + w - 10 * qa;
    u32x4 xa, xr[2];
    asm volatile("s_waitcnt lgkmcnt(0)" : "+s"(tb));
    if (w == 0) tw0 = (unsigned)KARG(84, 85);
    tb4[w] = (unsigned)tb;
    BAR();
    const int n1 = isq ? (0x62646 >> (4 * sp)) & 15 : 6;
    const int gb = s0 + 4 + (w >> 1), qb = (gb * 26) >> 8, rb = gb - 10 * qb;
    ARGS2();
    const bf16 *x = GPTR(const bf16, 64, 65);
    const unsigned tstart = origin();
    const rsrc_t rc = mkrs(reinterpret_cast<const void*>(pc0 & ~255ull), CODEB);
    const int cofs = 1024 * (blockIdx.x >> 3);
    {
      const rsrc_t rx = mkrs(x, NB * DM * 2);
      const int xo = ((j & 3) * DM + 32 * ((j >> 2) & 1) + 8 * g) * 2;
      const unsigned cv = CPE ? __builtin_amdgcn_raw_buffer_load_b32(rc, 4 * tid, cofs, 0) : 0u;
#pragma unroll
      for (int i = 0; i < 2; ++i) xr[i] = ldb(rx, (w * DM + 16 * (lane < 40 ? lane : 0) + 8 * i) * 2, 0);
      xa = ldb(rx, xo, 128 * ra);
      SB();
      const u32x2 xb = __builtin_amdgcn_raw_buffer_load_b64(rx, xo, 128 * rb + 8 * (w & 1), 0);
      const u32x2 ep = __builtin_amdgcn_raw_buffer_load_b64(rw, WS_EPOCH, 0, AUX_SC1);
      SB();
      {
        float v[16]; float sm = 0.f;
#pragma unroll
        for (int i = 0; i < 2; ++i)
#pragma unroll
          for (int k = 0; k < 4; ++k) { v[8 * i + 2 * k] = bflo(xr[i][k]); v[8 * i + 2 * k + 1] = bfhi(xr[i][k]); sm += v[8 * i + 2 * k] + v[8 * i + 2 * k + 1]; }
        const float mean = wave_sum(lane < 40 ? sm : 0.f) * (1.f / DM);
        float sq = 0.f;
#pragma unroll
        for (int k = 0; k < 16; ++k) { v[k] -= mean; sq += v[k] * v[k]; }
        const float var = wave_sum(lane < 40 ? sq : 0.f) * (1.f / DM);
        stat[w][0] = rbf(mean); stat[w][1] = 1.f / rbf(sqrtf(rbf(rbf(var) + 1e-5f)));
        unsigned fv, fs;
        asm volatile("s_mov_b64 exec, 1\n\tds_add_u32 %2, %3\n\ts_mov_b64 exec, -1\n1%=:\n\tds_read_b32 %0, %2\n\ts_waitcnt lgkmcnt(0)\n\tv_readfirstlane_b32 %1, %0\n\ts_cmp_lt_u32 %1, 4\n\ts_cbranch_scc1 1%=b"
                     : "=&v"(fv), "=&s"(fs) : "v"((unsigned)(uintptr_t)&ctl[7]), "v"(lane == 0 ? 1u : 0u) : "scc", "memory");
      }
      SB();
      const float mean = stat[j & 3][0], rinv = stat[j & 3][1];
      auto lnx = [&](unsigned v) { const unsigned c = cvtpk(bflo(v) - mean, bfhi(v) - mean); return cvtpk(bflo(c) * rinv, bfhi(c) * rinv); };
#pragma unroll
      for (int st = 0; st < 2; ++st) {
        f32x4 d[8];
        if (st == 0) {
          const u32x4 af = {lnx(xa[0]), lnx(xa[1]), lnx(xa[2]), lnx(xa[3])};
#pragma unroll
          for (int ec = 0; ec < 8; ++ec) d[ec] = mfma(af, bfrag(&wv[0], ec), f32x4{0.f, 0.f, 0.f, 0.f});
        } else {
          const u32x2 af = {lnx(xb[0]), lnx(xb[1])};
#pragma unroll
          for (int ec = 0; ec < 8; ++ec) {
            const u32x2 bf = {__builtin_amdgcn_perm(wv[9][ec >> 1], wv[8][ec >> 1], (ec & 1) ? 0x07060302u : 0x05040100u),
                              __builtin_amdgcn_perm(wv[11][ec >> 1], wv[10][ec >> 1], (ec & 1) ? 0x07060302u : 0x05040100u)};
            d[ec] = __builtin_amdgcn_mfma_f32_16x16x16bf16_1k(__builtin_bit_cast(s16x4, af), __builtin_bit_cast(s16x4, bf), f32x4{0.f, 0.f, 0.f, 0.f}, 0, 0, 0);
          }
        }
        if (g == (j >> 3)) {
#pragma unroll
          for (int bb = 0; bb < NB; ++bb) {
            *reinterpret_cast<f32x4*>(&red[2 * (w + 4 * st) + g][bb][8 * (j & 7)]) = f32x4{d[0][bb], d[1][bb], d[2][bb], d[3][bb]};
            *reinterpret_cast<f32x4*>(&red[2 * (w + 4 * st) + g][bb][8 * (j & 7) + 4]) = f32x4{d[4][bb], d[5][bb], d[6][bb], d[7][bb]};
          }
        }
      }
      epoch = ((u64)__builtin_amdgcn_readfirstlane(ep[0]) | ((u64)__builtin_amdgcn_readfirstlane(ep[1]) << 32)) + 1ull;
      tag.t0 = (unsigned)epoch << 12;
      tag.t1 = ((unsigned)epoch * 0x9E377u ^ (unsigned)(epoch >> 20)) << 12;
      if (tid == 0) { ctl[0] = tag.t0; ctl[1] = tag.t1; }
      LDSINC(ctl[2]);
      asm volatile("" :: "v"(cv));
      if (w == 0) {
        LDSWAIT(ctl[2], 4, 0);
        constexpr int QS = NB * NH * DH * 4;
        const int qo = WS_Q + (g * NH * DH + h * DH + 4 * j) * 4;
        {
          f32x4 va = {0.f, 0.f, 0.f, 0.f}, vb = {0.f, 0.f, 0.f, 0.f};
#pragma unroll
          for (int p = 0; p < 8; ++p) {
            const f32x4 v = *reinterpret_cast<const f32x4*>(&red[2 * p][g][4 * j]) + *reinterpret_cast<const f32x4*>(&red[2 * p + 1][g][4 * j]);
            const f32x4 z = {0.f, 0.f, 0.f, 0.f};
            va += min(p, 4) < n1 ? v : z; vb += min(p, 4) >= n1 ? v : z;
          }
          const int dst = isq ? qo + ((0x12110 >> (4 * sp)) & 15) * QS + (sp >> 1) * 256
                              : WS_GU + (((fu & 1) * NB + g) * (2 * FF) + 64 * cb + 4 * j) * 4;
          const u32x4 pa = p_pack(va, tag);
          if (PTL) {
            unsigned due = tstart + TOFS + sched(PTL) + POF + ((h * SPAN) >> 5);
            if ((int)((unsigned)__builtin_amdgcn_s_memrealtime() - due) > PWN) due += TAU;
            while ((int)((unsigned)__builtin_amdgcn_s_memrealtime() - due) < 0) __builtin_amdgcn_s_sleep(1);
          }
          st_ag(rw, dst, pa);
          if (isq && lane < 8) st_ag(rw, WS_OR + h * 1024 + 128 * sp + 16 * lane, u32x4{tstart, tag.t0, 0u, tag.t1});
          if (n1 < 6) st_ag(rw, qo + ((sp + 1) >> 1) * 256, p_pack(vb, tag));
        }
        while ((unsigned)__builtin_amdgcn_s_memrealtime() - tstart < (unsigned)(TOFS + sched(QTL) + QOF + ((h * SPAN) >> 5))) __builtin_amdgcn_s_sleep(2);
        u32x4 ga[3], gb[3], gc, gd;
#pragma unroll 1
        for (int it = 0; it < SPIN_MAX; ++it) {
          FENCE();
          bool ok = true;
#pragma unroll
          for (int i = 0; i < 3; ++i) { ga[i] = ld_ag(rw, qo + 256 * i); gb[i] = ld_ag(rw, qo + QS + 256 * i); }
          gc = ld_ag(rw, qo + 2 * QS + 256);
          gd = ld_ag(rw, WS_OR + h * 1024 + 128 * min(lane, 4));
#pragma unroll
          for (int i = 0; i < 3; ++i) ok = ok && g_ok(ga[i], tag) && g_ok(gb[i], tag);
          if (__all(ok && g_ok(gc, tag) && g_ok(gd, tag))) break;
          __builtin_amdgcn_s_sleep(QBACK);
        }
#pragma unroll
        for (int i = 0; i < 3; ++i) {
          f32x4 qa = p_val(ga[i]) + p_val(gb[i]);
          if (i == 1) qa += p_val(gc);
          *reinterpret_cast<u32x2*>(&qs[g][2 * j + 32 * i]) = u32x2{cvtpk(qa[0], qa[1]), cvtpk(qa[2], qa[3])};
        }
        {
          const unsigned o0 = __builtin_amdgcn_readfirstlane(gd[0]);
          int dm = 0;
#pragma unroll
          for (int i = 1; i < 5; ++i) dm += (int)(__builtin_amdgcn_readlane(gd[0], i) - o0);
          ctl[15] = tstart + min(max((int)(o0 - tstart) + ((dm * 13) >> 6), -100), 200);
        }
        LDSINC(ctl[13]);
      }
    }
    mkrole();
    if (role - 161u < 40u) {
      const int hp = (role - 1) / 5;
      if (w < 3) {
        const int go = WS_GU + (g * (2 * FF) + DH * (hp - NH) + 64 * w + 4 * j) * 4;
        u32x4 g0, g1, g2, g3;
        while ((unsigned)__builtin_amdgcn_s_memrealtime() - tstart < (unsigned)(TOFS + sched(HGT) + 2 * TAU2 * w + 15 + ((h * SPAN) >> 5))) __builtin_amdgcn_s_sleep(8);
#pragma unroll 1
        for (int it = 0; it < SPIN_MAX; ++it) {
          FENCE();
          g0 = ld_ag(rw, go); g1 = ld_ag(rw, go + FF * 4);
          g2 = ld_ag(rw, go + NB * 2 * FF * 4); g3 = ld_ag(rw, go + NB * 2 * FF * 4 + FF * 4);
          if (__all(g_ok(g0, tag) && g_ok(g1, tag) && g_ok(g2, tag) && g_ok(g3, tag))) break;
          __builtin_amdgcn_s_sleep(32);
        }
        const f32x4 gt = p_val(g0) + p_val(g2), up = p_val(g1) + p_val(g3);
        f32x4 hv;
#pragma unroll
        for (int i = 0; i < 4; ++i) { const float gg = rbf(gt[i]); hv[i] = rbf(gg / (1.f + __expf(-gg))) * rbf(up[i]); }
        *reinterpret_cast<u32x2*>(&oA[g][32 * w + 2 * j]) = u32x2{cvtpk(hv[0], hv[1]), cvtpk(hv[2], hv[3])};
      } else {
        if (lane < DH / 4) *reinterpret_cast<u32x2*>(&oA[4][2 * lane]) = u32x2{0u, 0u};
      }
      LDSADD(ctl[11], 6u);
    }
    if (NCK && w == 3) {
      int ofv = 0;
      { const unsigned pb = __builtin_amdgcn_raw_buffer_load_b32(rw, WS_BC + 4096 * (lane >> 4) + 512 * (h & 7) + 128 * (lane & 3), 0, AUX_SC1); asm volatile("" :: "v"(pb)); }
      const int t5 = min(lane, 31);
      const unsigned due = tstart + TOFS + ((h * SPAN) >> 5) + sched(t5);
#pragma unroll 1
      for (int k = 0; k < NCK; ++k) {
        const int c = CK0 + k * CKP;
        if (sp == RSP) {
          const unsigned t1 = __builtin_amdgcn_readlane(due + ofv, c) + 30;
          while ((int)((unsigned)__builtin_amdgcn_s_memrealtime() - t1) < 0) __builtin_amdgcn_s_sleep(2);
          FENCE();
          const u32x4 la = *reinterpret_cast<const u32x4*>(&lat[0]), lb = *reinterpret_cast<const u32x4*>(&lat[4]);
          const unsigned ls = (la[0] + la[1] + la[2] + la[3] + lb[0] + lb[1] + lb[2] + lb[3]) >> 3;
          if (ls >= LTH && lane < 8) st_ag(rw, WS_BC + 4096 * k + 512 * lane + 16 * h, u32x4{min(ls, 1000u), tag.t0, 0u, tag.t1});
        }
        const unsigned t2 = __builtin_amdgcn_readlane(due + ofv, c + CKR) + 15;
        while ((int)((unsigned)__builtin_amdgcn_s_memrealtime() - t2) < 0) __builtin_amdgcn_s_sleep(2);
        FENCE();
        const u32x4 mk = ld_ag(rw, WS_BC + 4096 * k + 512 * (h & 7) + 16 * t5);
        const int sm = (int)wave_sum(g_ok(mk, tag) && lane < 32 ? (float)mk[0] : 0.f);
        const int need = ((sm * KG) >> 9) - (__builtin_amdgcn_readlane(ofv, min(c + CKE + CKS - 1, 31)) - __builtin_amdgcn_readlane(ofv, c));
        ofv += min(max(need, 0) / CKS, DMX) * min(max(t5 - (c + CKE) + 1, 0), CKS);
        if (KI && need >= KI) ofv += max(t5 - (c + CKE + CKS) + 1, 0);
        if (lane < 32) ofs[lane] = ofv;
      }
    }
    if (w == 1) {
      LDSWAIT(ctl[4], 8, 16);
      const float mi = mlw[0][lane & 31], li = mlw[1][lane & 31];
      float M = fmaxf(mi, dpp<XOR1>(mi)); M = fmaxf(M, dpp<XOR2>(M)); M = rnd22(fmaxf(M, dpp<XOR7>(M)));
      const float fw = __builtin_amdgcn_exp2f(mi - M);
      mlw[3][lane & 31] = fw;
      LDSINC(ctl[12]);
      float L = li * fw;
      L += dpp<XOR1>(L); L += dpp<XOR2>(L); L += dpp<XOR7>(L);
      const u32x4 gp = g_pack(M, L, dpp<XOR8>(M), dpp<XOR8>(L), tag);
      if ((lane & 44) == 0) st_ag(rw, WS_ML + (h * 8 + sp) * 128 + (lane & 16) + 32 * (lane & 3), lane & 3 ? u32x4{ctl[15], tag.t0, 0u, tag.t1} : gp);
    }
    if (role <= 200) {
      const int hp = (role - 1) / 5;
      if (w == 1 && role <= 160) {
        while ((unsigned)__builtin_amdgcn_s_memrealtime() - tstart < (unsigned)sched(24) + TOFS + MLD + ofs[24]) __builtin_amdgcn_s_sleep(8);
        u32x4 g0;
#pragma unroll 1
        for (int it = 0; it < SPIN_MAX; ++it) {
          FENCE();
          g0 = ld_ag(rw, WS_ML + (hp * 8 + (lane & 7)) * 128 + (lane & 48));
          if (__all(g_ok(g0, tag))) break;
          __builtin_amdgcn_s_sleep(12);
        }
        const f32x4 a0 = g_val(g0);
        const float mq = (lane & 8) ? a0[2] : a0[0], lq = (lane & 8) ? a0[3] : a0[1];
        float Mx = fmaxf(mq, dpp<XOR1>(mq)); Mx = fmaxf(Mx, dpp<XOR2>(Mx)); Mx = fmaxf(Mx, dpp<XOR7>(Mx));
        const float e0 = __builtin_amdgcn_exp2f(mq - Mx);
        float lt = e0 * lq; lt += dpp<XOR1>(lt); lt += dpp<XOR2>(lt); lt += dpp<XOR7>(lt);
        if (lane < 32) mlw[2][lane] = e0 / lt * (2.f / 511.f);
        if (lane == 32) ctl[3] = g0[0];
        LDSINC(ctl[9]);
      }
      typedef __attribute__((address_space(3))) const u32x4* lpu;
      typedef __attribute__((address_space(3))) s16x4* lpt;
      int ydst = WS_Y + ((role - 1 - 5 * hp) * (NH + 8) + hp) * 2048 + w * 512 + (j & 3) * 128 + ((j >> 3) * 4 + g) * 16;
      unsigned ard = (unsigned)(uintptr_t)&oA[role <= 160 ? j : min(j, 4)][4 * g];
      unsigned wr0 = (unsigned)(uintptr_t)&wf[j >> 2][0] + 8 * (j & 1) + 2048 * g + 16 * ((4 * w + ((j >> 1) & 1)) ^ (j & 12) ^ ((2 * g) & 3));
      u32x4 rq = {(unsigned)(uintptr_t)ws, (unsigned)((uintptr_t)ws >> 32) & 0xffffu, (unsigned)WS_END, 0x00020000u};
      u64 ym = 0x0f0f0f0f0f0f0f0full;
      asm volatile("" : "+v"(ydst), "+v"(ard), "+v"(wr0)); asm volatile("" : "+s"(rq)); asm volatile("" : "+s"(ym));
      auto trd = [](unsigned a) { return __builtin_bit_cast(u32x2, __builtin_amdgcn_ds_read_tr16_b64_v4i16(reinterpret_cast<lpt>(a))); };
      auto frag = [&](int n, int ks) { const u32x2 lo = trd((wr0 ^ (32 * n)) + 8192 * ks), hi = trd((wr0 ^ (32 * n + 16)) + 8192 * ks + 1024);
                                       return u32x4{lo[0], lo[1], hi[0], hi[1]}; };
      u32x4 bw[2][6], af[6];
      f32x4 y[2] = {{0.f, 0.f, 0.f, 0.f}, {0.f, 0.f, 0.f, 0.f}};
      LDSWAIT(ctl[6], 2, 1);
#pragma unroll
      for (int ks = 0; ks < 4; ++ks)
#pragma unroll
        for (int n = 0; n < 2; ++n) bw[n][ks] = frag(n, ks);
#pragma unroll
      for (int ks = 0; ks < 4; ++ks) asm volatile("" : "+v"(bw[0][ks]), "+v"(bw[1][ks]));
      unsigned cn;
      LDSWAITV(cn, ctl[11], 24, 0);
#pragma unroll
      for (int ks = 0; ks < 6; ++ks) af[ks] = *reinterpret_cast<lpu>(ard + 64 * ks);
      SB();
      bw[0][4] = frag(0, 4); bw[1][4] = frag(1, 4); bw[0][5] = frag(0, 5); bw[1][5] = frag(1, 5);
      SB();
#pragma unroll
      for (int ks = 0; ks < 4; ++ks)
#pragma unroll
        for (int n = 0; n < 2; ++n) y[n] = mfma(bw[n][ks], af[ks], y[n]);
      SB();
      if (cn != 26) {
        LDSWAIT(ctl[11], 26, 0);
        bw[0][4] = frag(0, 4); bw[1][4] = frag(1, 4); bw[0][5] = frag(0, 5); bw[1][5] = frag(1, 5);
      }
#pragma unroll
      for (int ks = 4; ks < 6; ++ks)
#pragma unroll
        for (int n = 0; n < 2; ++n) y[n] = mfma(bw[n][ks], af[ks], y[n]);
      f32x4 z;
#define A8(d, s, m) "v_add_f32_dpp %" #d ", %" #s ", %" #s " row_ror:8 row_mask:0xf bank_mask:" #m "\n\t"
      asm volatile("s_nop 6\n\t" A8(0, 4, 0x3) A8(1, 5, 0x3) A8(2, 6, 0x3) A8(3, 7, 0x3) A8(0, 8, 0xc) A8(1, 9, 0xc) A8(2, 10, 0xc) A8(3, 11, 0xc)
                   : "=&v"(z[0]), "=&v"(z[1]), "=&v"(z[2]), "=&v"(z[3])
                   : "v"(y[0][0]), "v"(y[0][1]), "v"(y[0][2]), "v"(y[0][3]), "v"(y[1][0]), "v"(y[1][1]), "v"(y[1][2]), "v"(y[1][3]));
#pragma unroll
      for (int r = 0; r < 4; ++r) z[r] += dpp<0x104>(z[r]);
      const u32x4 yp = g_pack(z[0], z[1], z[2], z[3], tag);
      asm volatile("s_mov_b64 exec, %3\n\tbuffer_store_dwordx4 %0, %1, %2, 0 offen sc1\n\ts_mov_b64 exec, -1\n\ts_nop 0"
                   :: "v"(yp), "v"(ydst), "s"(rq), "s"(ym) : "memory");
      asm volatile("" :: "v"(y[0]), "v"(y[1]));
    }
    if (w == 2 && role == 256) {
      LDSWAIT(ctl[8], 3, 8);
#pragma unroll 1
      for (int it = 0; it < SPIN_MAX; ++it) {
        FENCE();
        bool ok = true;
#pragma unroll
        for (int i = 0; i < 4; ++i) ok = ok && g_ok(ld_ag(rw, WS_ML + (64 * i + lane) * 128), tag);
        if (__all(ok)) break;
        __builtin_amdgcn_s_sleep(32);
      }
      if (lane == 0) __hip_atomic_store(reinterpret_cast<u64*>(ws + WS_EPOCH), epoch, __ATOMIC_RELAXED, __HIP_MEMORY_SCOPE_AGENT);
    }
  }
}

extern "C" void kernel_launch(void* const* d_in, const int* in_sizes, int n_in, void* d_out, int out_size, void* d_ws,
                              size_t ws_size, hipStream_t stream) {
  fused_k<<<256, 768, 0, stream>>>((const bf16*)d_in[0], (const bf16*)d_in[1], (const bf16*)d_in[2], (const bf16*)d_in[3],
                                   (const bf16*)d_in[4], (const bf16*)d_in[6], (const bf16*)d_in[7], (const bf16*)d_in[8],
                                   (unsigned char*)d_ws, (bf16*)d_out);
}
```
